```python
import torch

def _softmax(x, axis=-1):
    return torch.softmax(x, dim=axis)
def _gelu(x):
    return torch.nn.functional.gelu(x, approximate="tanh")
def _silu(x):
    return torch.nn.functional.silu(x)
def _sigmoid(x):
    return torch.sigmoid(x)
def _layernorm(x, axis=-1, eps=1e-5):
    m = x.mean(dim=axis, keepdim=True)
    v = x.var(dim=axis, keepdim=True, unbiased=False)
    return (x - m) / torch.sqrt(v + eps)
def _scatter_add(tgt, idx, src, axis=0):
    return tgt.clone().index_add_(axis, idx, src)
def _segment_sum(src, seg, n):
    out = torch.zeros((n,) + src.shape[1:], dtype=src.dtype)
    return out.index_add_(0, seg, src)
def _pad(x, pw):
    flat = tuple(v for pair in reversed(pw) for v in pair)
    return torch.nn.functional.pad(x, flat)

kernel_name = 'standalone__edm_precond__diffusion'

def setup_inputs():

    _w = (lambda a: (lambda t: t.to(torch.bfloat16) if t.is_floating_point() else t)(torch.as_tensor(a)))
    B, C, HW = 2, 320, 128
    return {
        "x": _w((lambda *s: torch.randn(*s, dtype=torch.bfloat16) * 0.2)(B, C, HW, HW)),
        "sigma": _w((lambda _x: torch.exp(torch.as_tensor(_x)))((lambda *s: torch.randn(*s, dtype=torch.bfloat16) * 3.2)(B))),
        "w_in": _w((lambda *s: torch.randn(*s, dtype=torch.bfloat16) * 3.2)(C, C) * (C ** -0.5)),
        "w_emb": _w((lambda *s: torch.randn(*s, dtype=torch.bfloat16) * 3.2)(1, C)),
        "w_out": _w((lambda *s: torch.randn(*s, dtype=torch.bfloat16) * 3.2)(C, C) * (C ** -0.5)),
    }

def reference(x, sigma, w_in, w_emb, w_out):
    B, C, H, W = x.shape
    sigma_data = 0.5
    s = sigma.reshape(B, 1, 1, 1)
    denom = s * s + sigma_data * sigma_data
    c_skip = (sigma_data * sigma_data) / denom
    c_out  = s * sigma_data / torch.sqrt(denom)
    c_in   = 1.0 / torch.sqrt(denom)
    c_noise = 0.25 * torch.log(sigma).reshape(B, 1)
    h = torch.einsum("bchw,cd->bdhw", c_in * x, w_in)
    h = h + (c_noise @ w_emb).reshape(B, C, 1, 1)
    h = _silu(h)
    Fx = torch.einsum("bchw,cd->bdhw", h, w_out)
    return c_skip * x + c_out * Fx
```

```cpp
#include <hip/hip_runtime.h>
#include <hip/hip_bf16.h>
#include <stdint.h>

typedef unsigned short u16;
__device__ __forceinline__ float bf2f(u16 v){ return __uint_as_float(((unsigned)v)<<16); }
__device__ __forceinline__ u16 f2bf(float f){ __bf16 b = (__bf16)f; return __builtin_bit_cast(u16,b); }
__device__ __forceinline__ float rbf(float f){ return bf2f(f2bf(f)); }

constexpr int C=320, P=16384;

struct Coef { float c_skip, c_out, c_in, c_noise; };
__device__ Coef coefs(float s){
  Coef c;
  float denom = rbf(rbf(s*s)+0.25f);
  c.c_skip = rbf(0.25f/denom);
  float sq = rbf(sqrtf(denom));
  c.c_out = rbf(rbf(s*0.5f)/sq);
  c.c_in = rbf(1.0f/sq);
  c.c_noise = rbf(0.25f*rbf(logf(s)));
  return c;
}

__global__ void k1(const u16* x, const u16* sigma, const u16* w_in, const u16* w_emb, u16* h){
  int p = blockIdx.x*256+threadIdx.x; int d = blockIdx.y; int b = blockIdx.z;
  Coef cf = coefs(bf2f(sigma[b]));
  float acc=0;
  for(int c=0;c<C;c++) acc += bf2f(x[((size_t)b*C+c)*P+p]) * bf2f(w_in[c*C+d]);
  float v = cf.c_in*acc + rbf(cf.c_noise*bf2f(w_emb[d]));
  float s = v/(1.f+__expf(-v));
  h[((size_t)b*C+d)*P+p] = f2bf(s);
}
__global__ void k2(const u16* x, const u16* sigma, const u16* w_out, const u16* h, u16* out){
  int p = blockIdx.x*256+threadIdx.x; int d = blockIdx.y; int b = blockIdx.z;
  Coef cf = coefs(bf2f(sigma[b]));
  float acc=0;
  for(int c=0;c<C;c++) acc += bf2f(h[((size_t)b*C+c)*P+p]) * bf2f(w_out[c*C+d]);
  out[((size_t)b*C+d)*P+p] = f2bf(cf.c_skip*bf2f(x[((size_t)b*C+d)*P+p]) + cf.c_out*acc);
}

extern "C" void kernel_launch(void* const* d_in, const int* in_sizes, int n_in,
                              void* d_out, int out_size, void* d_ws, size_t ws_size,
                              hipStream_t stream) {
  const u16* x=(const u16*)d_in[0]; const u16* sigma=(const u16*)d_in[1];
  const u16* w_in=(const u16*)d_in[2]; const u16* w_emb=(const u16*)d_in[3]; const u16* w_out=(const u16*)d_in[4];
  dim3 g(P/256, C, 2);
  k1<<<g,256,0,stream>>>(x,sigma,w_in,w_emb,(u16*)d_ws);
  k2<<<g,256,0,stream>>>(x,sigma,w_out,(const u16*)d_ws,(u16*)d_out);
}
```

```cpp
#include <hip/hip_runtime.h>
#include <stdint.h>

typedef unsigned short u16;
typedef short s16x4 __attribute__((ext_vector_type(4)));
typedef short s16x8 __attribute__((ext_vector_type(8)));
typedef __bf16 bf16x8 __attribute__((ext_vector_type(8)));
typedef __bf16 bf16x2 __attribute__((ext_vector_type(2)));
typedef float f32x16 __attribute__((ext_vector_type(16)));
typedef float f32x4 __attribute__((ext_vector_type(4)));
typedef float f32x2 __attribute__((ext_vector_type(2)));
typedef unsigned u32x4 __attribute__((ext_vector_type(4)));
typedef __attribute__((address_space(3))) s16x4* lds_s16x4p;
typedef __attribute__((address_space(3))) u32x4* lds_u32x4p;
typedef __attribute__((address_space(3))) f32x4* lds_f32x4p;
typedef __attribute__((address_space(3))) float* lds_f32p;

#define DI __device__ __forceinline__
constexpr int C=320, P=16384;
constexpr int XIMG = 0;
constexpr int WRING = 81920;
constexpr int NSLOT = 3;
constexpr int BIAS = WRING + NSLOT*20480;
constexpr int LDS_BYTES = BIAS + 1280;

DI float bf2f(u16 v){ return __uint_as_float(((unsigned)v)<<16); }
DI u16 f2bf(float f){ __bf16 b = (__bf16)f; return __builtin_bit_cast(u16,b); }
DI float rbf(float f){ return bf2f(f2bf(f)); }
DI unsigned pk2(float lo, float hi){ f32x2 v={lo,hi}; bf16x2 r=__builtin_convertvector(v,bf16x2); return __builtin_bit_cast(unsigned,r); }

template<int OFF> DI void glds16(const void* sbase, unsigned voff, unsigned lds){
  asm volatile("s_mov_b32 m0, %2\n\ts_nop 0\n\tglobal_load_lds_dwordx4 %0, %1 offset:%3" :: "v"(voff), "s"(sbase), "s"(lds), "i"(OFF) : "memory");
}
template<int N> DI void waitv(){ asm volatile("s_waitcnt vmcnt(%0)" :: "i"(N) : "memory"); }
DI void waitl0(){ asm volatile("s_waitcnt lgkmcnt(0)" ::: "memory"); }
DI void bar(){ __builtin_amdgcn_s_barrier(); }
DI s16x4 trr(unsigned a){ return __builtin_amdgcn_ds_read_tr16_b64_v4i16((lds_s16x4p)(size_t)a); }
DI bf16x8 frag(unsigned a0, unsigned a1){ s16x4 lo=trr(a0), hi=trr(a1); return __builtin_bit_cast(bf16x8, __builtin_shufflevector(lo,hi,0,1,2,3,4,5,6,7)); }
#define MFMA(a,b,c) __builtin_amdgcn_mfma_f32_32x32x16_bf16((a),(b),(c),0,0,0)

DI unsigned xswz(unsigned row){ return (((row>>1)&1)<<3) | ((row&1)<<2) | (((row>>2)&1)<<1) | ((row>>1)&1); }

__global__ __launch_bounds__(256,1) void edm_kernel(const u16* __restrict__ x, const u16* __restrict__ sigma,
      const u16* __restrict__ w_in, const u16* __restrict__ w_emb, const u16* __restrict__ w_out, u16* __restrict__ out)
{
  extern __shared__ __attribute__((aligned(16))) char smem[];
  const unsigned lb = (unsigned)(size_t)smem;
  const int tid = threadIdx.x, lane = tid&63;
  const int wid = __builtin_amdgcn_readfirstlane(tid>>6);
  const int blk = blockIdx.x;
  const int b = blk>>7, p0 = (blk&127)*128;
  const char* xb = (const char*)x + ((size_t)b*C*P + p0)*2;
  char* ob = (char*)out + ((size_t)b*C*P + p0)*2;

  unsigned wv[5];
  #pragma unroll
  for (int i=0;i<5;i++){ unsigned o=i*1024+lane*16; unsigned row=o/640; wv[i]=(lane*16) ^ (((row>>1)&1)<<6); }
  unsigned xv[2];
  #pragma unroll
  for (int e=0;e<2;e++){ unsigned row = (2*wid+e)*4 + (lane>>4); xv[e] = row*(P*2) + (((lane&15) ^ xswz(row))<<4); }

  auto issueW = [&](int ch){
    const char* src = (const char*)(ch<10 ? w_in : w_out) + (ch%10)*20480 + wid*5120 + 2048;
    unsigned dst = lb + WRING + (ch%NSLOT)*20480 + wid*5120 + 2048;
    glds16<-2048>(src, wv[0], dst); glds16<-1024>(src, wv[1], dst); glds16<0>(src, wv[2], dst);
    glds16<1024>(src, wv[3], dst); glds16<2048>(src, wv[4], dst);
  };
  auto issueX = [&](int g){
    const char* src = xb + (size_t)g*32*P*2;
    unsigned dst = lb + XIMG + g*8192 + wid*2048;
    glds16<0>(src, xv[0], dst); glds16<0>(src, xv[1], dst+1024);
  };
  unsigned embraw;
  { unsigned eo = (tid<160?tid:159)*4; asm volatile("global_load_dword %0, %1, %2" : "=v"(embraw) : "v"(eo), "s"(w_emb) : "memory"); }
  issueW(0); issueX(0); issueW(1);
  #pragma unroll
  for (int g=1; g<10; g++) issueX(g);

  unsigned sraw = ((const unsigned*)sigma)[0];
  float s = __uint_as_float(b ? (sraw & 0xffff0000u) : (sraw<<16));
  float denom = rbf(rbf(s*s)+0.25f);
  float c_skip = rbf(0.25f/denom);
  float sq = rbf(sqrtf(denom));
  float c_out = rbf(rbf(s*0.5f)/sq);
  float c_in = rbf(1.0f/sq);
  float c_noise = rbf(0.25f*rbf(logf(s)));
  c_in = fmaxf(c_in, 1e-19f);
  float inv_cin = 1.0f/c_in;
  float tscale = -1.4426950408889634f*c_in;
  float invK = fminf(1.0f/(c_in*c_out), 1e37f);
  asm volatile("s_waitcnt vmcnt(30)" : "+v"(embraw) :: "memory");
  if (tid<160){ *(lds_f32p)(size_t)(lb+BIAS+tid*8) = rbf(c_noise*__uint_as_float(embraw<<16))*inv_cin;
                *(lds_f32p)(size_t)(lb+BIAS+tid*8+4) = rbf(c_noise*__uint_as_float(embraw&0xffff0000u))*inv_cin; }
  waitl0(); bar();

  const int h = lane>>5, blk16=(lane>>4)&1, q=(lane&15)>>2, p=lane&3;
  f32x16 acc[10];
  #pragma unroll
  for (int t=0;t<10;t++)
    #pragma unroll
    for (int g=0;g<4;g++){ f32x4 v = *(lds_f32x4p)(size_t)(lb+BIAS+(32*t+8*g+4*h)*4); acc[t][4*g]=v[0]; acc[t][4*g+1]=v[1]; acc[t][4*g+2]=v[2]; acc[t][4*g+3]=v[3]; }

  const unsigned wb = (q>>1)&1;
  const unsigned wrow = lb + WRING + (4*h+q)*640 + 32*blk16 + 16*(p>>1) + 8*(p&1);
  const unsigned wE = wrow + 64*wb, wO = wrow + 64*(1^wb);
  const unsigned xr = 4*h+q;
  const unsigned xA = lb + XIMG + xr*256 + (((4*wid + 2*blk16 + (p&1)) ^ xswz(xr))<<4) + 8*(p>>1);

  #pragma unroll
  for (int ch=0; ch<10; ch++){
    if (ch==0) waitv<23>();
    else if (ch==1) waitv<16+5>();
    else waitv<5>();
    waitl0(); bar();
    issueW(ch+2);
    #pragma unroll
    for (int s2=0;s2<2;s2++){
      bf16x8 xf = frag(xA + (ch*32+s2*16)*256, xA + (ch*32+s2*16+8)*256);
      #pragma unroll
      for (int t=0;t<10;t++){
        unsigned a = ((t&1)?wO:wE) + (ch%NSLOT)*20480 + (s2*16)*640 + 128*(t>>1);
        bf16x8 wf = frag(a, a+8*640);
        acc[t] = MFMA(wf, xf, acc[t]);
      }
    }
  }
  bf16x8 hf[20];
  #pragma unroll
  for (int t=0;t<10;t++){
    #pragma unroll
    for (int e=0;e<2;e++){
      u32x4 pk;
      #pragma unroll
      for (int j=0;j<4;j++){
        float r[2];
        #pragma unroll
        for (int u=0;u<2;u++){
          float a = acc[t][8*e+2*j+u];
          float ex = __builtin_amdgcn_exp2f(a*tscale);
          float dn = __builtin_fmaf(ex, invK, invK);
          r[u] = a*__builtin_amdgcn_rcpf(dn);
        }
        pk[j] = pk2(r[0], r[1]);
      }
      hf[2*t+e] = __builtin_bit_cast(bf16x8, pk);
    }
  }
  bf16x8 sk[2];
  {
    const u16 cs = f2bf(c_skip);
    #pragma unroll
    for (int e=0;e<2;e++){ s16x8 v;
      #pragma unroll
      for (int j=0;j<8;j++) v[j] = ((lane&31) == 16*e+8*(j>>2)+4*h+(j&3)) ? (short)cs : (short)0;
      sk[e]=__builtin_bit_cast(bf16x8,v); }
  }
  #pragma unroll
  for (int t=0;t<10;t++){
    f32x16 z = {};
    bf16x8 x0 = frag(xA + (t*32)*256, xA + (t*32+8)*256);
    bf16x8 x1 = frag(xA + (t*32+16)*256, xA + (t*32+24)*256);
    z = MFMA(x0, sk[0], z);
    acc[t] = MFMA(x1, sk[1], z);
  }
  #pragma unroll
  for (int ch=10; ch<20; ch++){
    if (ch<19) waitv<5>(); else waitv<0>();
    waitl0(); bar();
    if (ch+2<20) issueW(ch+2);
    #pragma unroll
    for (int s2=0;s2<2;s2++){
      #pragma unroll
      for (int t=0;t<10;t++){
        unsigned a = ((t&1)?wO:wE) + (ch%NSLOT)*20480 + (s2*16)*640 + 128*(t>>1);
        bf16x8 wf = frag(a, a+8*640);
        acc[t] = MFMA(hf[(ch-10)*2+s2], wf, acc[t]);
      }
    }
  }
  {
    const unsigned d = lane&31;
    #pragma unroll
    for (int t=0;t<10;t++){
      #pragma unroll
      for (int e=0;e<2;e++){
        u32x4 pk;
        #pragma unroll
        for (int j=0;j<4;j++) pk[j] = pk2(acc[t][8*e+2*j], acc[t][8*e+2*j+1]);
        unsigned a = lb + XIMG + (32*t+d)*256 + (((4*wid + 2*e + h) ^ xswz(d))<<4);
        *(lds_u32x4p)(size_t)a = pk;
      }
    }
    waitl0(); bar();
    #pragma unroll
    for (int g=0; g<10; g++)
      #pragma unroll
      for (int e=0;e<2;e++){
        u32x4 v = *(lds_u32x4p)(size_t)(lb + XIMG + g*8192 + wid*2048 + e*1024 + lane*16);
        *(u32x4*)(ob + (size_t)g*32*P*2 + xv[e]) = v;
      }
  }
}

extern "C" void kernel_launch(void* const* d_in, const int* in_sizes, int n_in,
                              void* d_out, int out_size, void* d_ws, size_t ws_size,
                              hipStream_t stream) {
  (void)hipFuncSetAttribute((const void*)edm_kernel, hipFuncAttributeMaxDynamicSharedMemorySize, LDS_BYTES);
  edm_kernel<<<256,256,LDS_BYTES,stream>>>((const u16*)d_in[0],(const u16*)d_in[1],(const u16*)d_in[2],(const u16*)d_in[3],(const u16*)d_in[4],(u16*)d_out);
}
```

```cpp
#include <hip/hip_runtime.h>
#include <stdint.h>

typedef unsigned short u16;
typedef short s16x4 __attribute__((ext_vector_type(4)));
typedef short s16x8 __attribute__((ext_vector_type(8)));
typedef __bf16 bf16x8 __attribute__((ext_vector_type(8)));
typedef __bf16 bf16x2 __attribute__((ext_vector_type(2)));
typedef float f32x16 __attribute__((ext_vector_type(16)));
typedef float f32x4 __attribute__((ext_vector_type(4)));
typedef float f32x2 __attribute__((ext_vector_type(2)));
typedef unsigned u32x4 __attribute__((ext_vector_type(4)));
typedef __attribute__((address_space(3))) s16x4* lds_s16x4p;
typedef __attribute__((address_space(3))) u32x4* lds_u32x4p;
typedef __attribute__((address_space(3))) f32x4* lds_f32x4p;
typedef __attribute__((address_space(3))) float* lds_f32p;

#define DI __device__ __forceinline__
constexpr int C=320, P=16384;
constexpr int XIMG = 0;
constexpr int WRING = 81920;
constexpr int NSLOT = 3;
constexpr int BIAS = WRING + NSLOT*20480;
constexpr int LDS_BYTES = BIAS + 1280;

DI float bf2f(u16 v){ return __uint_as_float(((unsigned)v)<<16); }
DI u16 f2bf(float f){ __bf16 b = (__bf16)f; return __builtin_bit_cast(u16,b); }
DI float rbf(float f){ return bf2f(f2bf(f)); }
DI unsigned pk2(float lo, float hi){ f32x2 v={lo,hi}; bf16x2 r=__builtin_convertvector(v,bf16x2); return __builtin_bit_cast(unsigned,r); }

template<int OFF> DI void glds16(const void* sbase, unsigned voff, unsigned lds){
  asm volatile("s_mov_b32 m0, %2\n\ts_nop 0\n\tglobal_load_lds_dwordx4 %0, %1 offset:%3" :: "v"(voff), "s"(sbase), "s"(lds), "i"(OFF) : "memory");
}
template<int N> DI void waitv(){ asm volatile("s_waitcnt vmcnt(%0)" :: "i"(N) : "memory"); }
DI void waitl0(){ asm volatile("s_waitcnt lgkmcnt(0)" ::: "memory"); }
DI void bar(){ __builtin_amdgcn_s_barrier(); }
DI s16x4 trr(unsigned a){ return __builtin_amdgcn_ds_read_tr16_b64_v4i16((lds_s16x4p)(size_t)a); }
DI bf16x8 frag(unsigned a0, unsigned a1){ s16x4 lo=trr(a0), hi=trr(a1); return __builtin_bit_cast(bf16x8, __builtin_shufflevector(lo,hi,0,1,2,3,4,5,6,7)); }
#define MFMA(a,b,c) __builtin_amdgcn_mfma_f32_32x32x16_bf16((a),(b),(c),0,0,0)

DI unsigned xswz(unsigned row){ return (((row>>1)&1)<<3) | ((row&1)<<2) | (((row>>2)&1)<<1) | ((row>>1)&1); }

__global__ __launch_bounds__(256,1) void edm_kernel(const u16* __restrict__ x, const u16* __restrict__ sigma,
      const u16* __restrict__ w_in, const u16* __restrict__ w_emb, const u16* __restrict__ w_out, u16* __restrict__ out)
{
  extern __shared__ __attribute__((aligned(16))) char smem[];
  const unsigned lb = (unsigned)(size_t)smem;
  const int tid = threadIdx.x, lane = tid&63;
  const int wid = __builtin_amdgcn_readfirstlane(tid>>6);
  const int blk = blockIdx.x;
  const int b = blk>>7, p0 = (blk&127)*128;
  const char* xb = (const char*)x + ((size_t)b*C*P + p0)*2;
  char* ob = (char*)out + ((size_t)b*C*P + p0)*2;

  { unsigned v=threadIdx.x; unsigned off = (tid&63)*16 + wid*1024; u32x4 t0;
    asm volatile("s_getpc_b64 s[40:41]\n"
      ".rept 16\n global_load_dwordx4 %0, %1, s[40:41]\n v_add_u32 %1, 4096, %1\n .endr\n"
      ""
      : "=&v"(t0), "+v"(off) :: "s40","s41","memory");
    asm volatile(".rept 4096\n v_add3_u32 %0, %0, %0, %0\n .endr\n s_waitcnt vmcnt(0)" : "+v"(v), "+v"(t0)); if (v==0x12345) out[0]=1; }
  unsigned wv[5];
  #pragma unroll
  for (int i=0;i<5;i++){ unsigned o=i*1024+lane*16; unsigned row=o/640; wv[i]=(lane*16) ^ (((row>>1)&1)<<6); }
  unsigned xv[2];
  #pragma unroll
  for (int e=0;e<2;e++){ unsigned row = (2*wid+e)*4 + (lane>>4); xv[e] = row*(P*2) + (((lane&15) ^ xswz(row))<<4); }

  auto issueW = [&](int ch){
    const char* src = (const char*)(ch<10 ? w_in : w_out) + (ch%10)*20480 + wid*5120 + 2048;
    unsigned dst = lb + WRING + (ch%NSLOT)*20480 + wid*5120 + 2048;
    glds16<-2048>(src, wv[0], dst); glds16<-1024>(src, wv[1], dst); glds16<0>(src, wv[2], dst);
    glds16<1024>(src, wv[3], dst); glds16<2048>(src, wv[4], dst);
  };
  auto issueX = [&](int g){
    const char* src = xb + (size_t)g*32*P*2;
    unsigned dst = lb + XIMG + g*8192 + wid*2048;
    glds16<0>(src, xv[0], dst); glds16<0>(src, xv[1], dst+1024);
  };
  unsigned embraw;
  { unsigned eo = (tid<160?tid:159)*4; asm volatile("global_load_dword %0, %1, %2" : "=v"(embraw) : "v"(eo), "s"(w_emb) : "memory"); }
  issueW(0); issueX(0); issueW(1);
  #pragma unroll
  for (int g=1; g<10; g++) issueX(g);

  unsigned sraw = ((const unsigned*)sigma)[0];
  float s = __uint_as_float(b ? (sraw & 0xffff0000u) : (sraw<<16));
  float denom = rbf(rbf(s*s)+0.25f);
  float c_skip = rbf(0.25f/denom);
  float sq = rbf(sqrtf(denom));
  float c_out = rbf(rbf(s*0.5f)/sq);
  float c_in = rbf(1.0f/sq);
  float c_noise = rbf(0.25f*rbf(logf(s)));
  c_in = fmaxf(c_in, 1e-19f);
  float inv_cin = 1.0f/c_in;
  float tscale = -1.4426950408889634f*c_in;
  float invK = fminf(1.0f/(c_in*c_out), 1e37f);
  asm volatile("s_waitcnt vmcnt(30)" : "+v"(embraw) :: "memory");
  if (tid<160){ *(lds_f32p)(size_t)(lb+BIAS+tid*8) = rbf(c_noise*__uint_as_float(embraw<<16))*inv_cin;
                *(lds_f32p)(size_t)(lb+BIAS+tid*8+4) = rbf(c_noise*__uint_as_float(embraw&0xffff0000u))*inv_cin; }
  waitl0(); bar();

  const int h = lane>>5, blk16=(lane>>4)&1, q=(lane&15)>>2, p=lane&3;
  f32x16 acc[10];
  #pragma unroll
  for (int t=0;t<10;t++)
    #pragma unroll
    for (int g=0;g<4;g++){ f32x4 v = *(lds_f32x4p)(size_t)(lb+BIAS+(32*t+8*g+4*h)*4); acc[t][4*g]=v[0]; acc[t][4*g+1]=v[1]; acc[t][4*g+2]=v[2]; acc[t][4*g+3]=v[3]; }

  const unsigned wb = (q>>1)&1;
  const unsigned wrow = lb + WRING + (4*h+q)*640 + 32*blk16 + 16*(p>>1) + 8*(p&1);
  const unsigned wE = wrow + 64*wb, wO = wrow + 64*(1^wb);
  const unsigned xr = 4*h+q;
  const unsigned xA = lb + XIMG + xr*256 + (((4*wid + 2*blk16 + (p&1)) ^ xswz(xr))<<4) + 8*(p>>1);

  #pragma unroll
  for (int ch=0; ch<10; ch++){
    if (ch==0) waitv<23>();
    else if (ch==1) waitv<16+5>();
    else waitv<5>();
    waitl0(); bar();
    issueW(ch+2);
    #pragma unroll
    for (int s2=0;s2<2;s2++){
      bf16x8 xf = frag(xA + (ch*32+s2*16)*256, xA + (ch*32+s2*16+8)*256);
      #pragma unroll
      for (int t=0;t<10;t++){
        unsigned a = ((t&1)?wO:wE) + (ch%NSLOT)*20480 + (s2*16)*640 + 128*(t>>1);
        bf16x8 wf = frag(a, a+8*640);
        acc[t] = MFMA(wf, xf, acc[t]);
      }
    }
  }
  bf16x8 hf[20];
  #pragma unroll
  for (int t=0;t<10;t++){
    #pragma unroll
    for (int e=0;e<2;e++){
      u32x4 pk;
      #pragma unroll
      for (int j=0;j<4;j++){
        float r[2];
        #pragma unroll
        for (int u=0;u<2;u++){
          float a = acc[t][8*e+2*j+u];
          float ex = __builtin_amdgcn_exp2f(a*tscale);
          float dn = __builtin_fmaf(ex, invK, invK);
          r[u] = a*__builtin_amdgcn_rcpf(dn);
        }
        pk[j] = pk2(r[0], r[1]);
      }
      hf[2*t+e] = __builtin_bit_cast(bf16x8, pk);
    }
  }
  bf16x8 sk[2];
  {
    const u16 cs = f2bf(c_skip);
    #pragma unroll
    for (int e=0;e<2;e++){ s16x8 v;
      #pragma unroll
      for (int j=0;j<8;j++) v[j] = ((lane&31) == 16*e+8*(j>>2)+4*h+(j&3)) ? (short)cs : (short)0;
      sk[e]=__builtin_bit_cast(bf16x8,v); }
  }
  #pragma unroll
  for (int t=0;t<10;t++){
    f32x16 z = {};
    bf16x8 x0 = frag(xA + (t*32)*256, xA + (t*32+8)*256);
    bf16x8 x1 = frag(xA + (t*32+16)*256, xA + (t*32+24)*256);
    z = MFMA(x0, sk[0], z);
    acc[t] = MFMA(x1, sk[1], z);
  }
  #pragma unroll
  for (int ch=10; ch<20; ch++){
    if (ch<19) waitv<5>(); else waitv<0>();
    waitl0(); bar();
    if (ch+2<20) issueW(ch+2);
    #pragma unroll
    for (int s2=0;s2<2;s2++){
      #pragma unroll
      for (int t=0;t<10;t++){
        unsigned a = ((t&1)?wO:wE) + (ch%NSLOT)*20480 + (s2*16)*640 + 128*(t>>1);
        bf16x8 wf = frag(a, a+8*640);
        acc[t] = MFMA(hf[(ch-10)*2+s2], wf, acc[t]);
      }
    }
  }
  {
    const unsigned d = lane&31;
    #pragma unroll
    for (int t=0;t<10;t++){
      #pragma unroll
      for (int e=0;e<2;e++){
        u32x4 pk;
        #pragma unroll
        for (int j=0;j<4;j++) pk[j] = pk2(acc[t][8*e+2*j], acc[t][8*e+2*j+1]);
        unsigned a = lb + XIMG + (32*t+d)*256 + (((4*wid + 2*e + h) ^ xswz(d))<<4);
        *(lds_u32x4p)(size_t)a = pk;
      }
    }
    waitl0(); bar();
    #pragma unroll
    for (int g=0; g<10; g++)
      #pragma unroll
      for (int e=0;e<2;e++){
        u32x4 v = *(lds_u32x4p)(size_t)(lb + XIMG + g*8192 + wid*2048 + e*1024 + lane*16);
        *(u32x4*)(ob + (size_t)g*32*P*2 + xv[e]) = v;
      }
  }
}

extern "C" void kernel_launch(void* const* d_in, const int* in_sizes, int n_in,
                              void* d_out, int out_size, void* d_ws, size_t ws_size,
                              hipStream_t stream) {
  (void)hipFuncSetAttribute((const void*)edm_kernel, hipFuncAttributeMaxDynamicSharedMemorySize, LDS_BYTES);
  edm_kernel<<<256,256,LDS_BYTES,stream>>>((const u16*)d_in[0],(const u16*)d_in[1],(const u16*)d_in[2],(const u16*)d_in[3],(const u16*)d_in[4],(u16*)d_out);
}
```

```cpp
#include <hip/hip_runtime.h>
#include <stdint.h>

typedef unsigned short u16;
typedef short s16x4 __attribute__((ext_vector_type(4)));
typedef short s16x8 __attribute__((ext_vector_type(8)));
typedef __bf16 bf16x8 __attribute__((ext_vector_type(8)));
typedef __bf16 bf16x2 __attribute__((ext_vector_type(2)));
typedef float f32x16 __attribute__((ext_vector_type(16)));
typedef float f32x4 __attribute__((ext_vector_type(4)));
typedef float f32x2 __attribute__((ext_vector_type(2)));
typedef unsigned u32x4 __attribute__((ext_vector_type(4)));
typedef __attribute__((address_space(3))) s16x4* lds_s16x4p;
typedef __attribute__((address_space(3))) u32x4* lds_u32x4p;
typedef __attribute__((address_space(3))) f32x4* lds_f32x4p;
typedef __attribute__((address_space(3))) float* lds_f32p;

#define DI __device__ __forceinline__
constexpr int C=320, P=16384;
constexpr int XIMG = 0;
constexpr int WRING = 81920;
constexpr int NSLOT = 3;
constexpr int BIAS = WRING + NSLOT*20480;
constexpr int LDS_BYTES = BIAS + 1280;

DI float bf2f(u16 v){ return __uint_as_float(((unsigned)v)<<16); }
DI u16 f2bf(float f){ __bf16 b = (__bf16)f; return __builtin_bit_cast(u16,b); }
DI float rbf(float f){ return bf2f(f2bf(f)); }
DI unsigned pk2(float lo, float hi){ f32x2 v={lo,hi}; bf16x2 r=__builtin_convertvector(v,bf16x2); return __builtin_bit_cast(unsigned,r); }

template<int OFF> DI void glds16(const void* sbase, unsigned voff, unsigned lds){
  asm volatile("s_mov_b32 m0, %2\n\ts_nop 0\n\tglobal_load_lds_dwordx4 %0, %1 offset:%3" :: "v"(voff), "s"(sbase), "s"(lds), "i"(OFF) : "memory");
}
template<int OFF> DI void glds16nt(const void* sbase, unsigned voff, unsigned lds){
  asm volatile("s_mov_b32 m0, %2\n\ts_nop 0\n\tglobal_load_lds_dwordx4 %0, %1 offset:%3 nt" :: "v"(voff), "s"(sbase), "s"(lds), "i"(OFF) : "memory");
}
template<int N> DI void waitv(){ asm volatile("s_waitcnt vmcnt(%0)" :: "i"(N) : "memory"); }
DI void waitl0(){ asm volatile("s_waitcnt lgkmcnt(0)" ::: "memory"); }
DI void bar(){ __builtin_amdgcn_s_barrier(); }
DI s16x4 trr(unsigned a){ return __builtin_amdgcn_ds_read_tr16_b64_v4i16((lds_s16x4p)(size_t)a); }
DI bf16x8 frag(unsigned a0, unsigned a1){ s16x4 lo=trr(a0), hi=trr(a1); return __builtin_bit_cast(bf16x8, __builtin_shufflevector(lo,hi,0,1,2,3,4,5,6,7)); }
#define MFMA(a,b,c) __builtin_amdgcn_mfma_f32_32x32x16_bf16((a),(b),(c),0,0,0)

DI unsigned xswz(unsigned row){ return (((row>>1)&1)<<3) | ((row&1)<<2) | (((row>>2)&1)<<1) | ((row>>1)&1); }

__global__ __launch_bounds__(256,1) void edm_kernel(const u16* __restrict__ x, const u16* __restrict__ sigma,
      const u16* __restrict__ w_in, const u16* __restrict__ w_emb, const u16* __restrict__ w_out, u16* __restrict__ out)
{
  extern __shared__ __attribute__((aligned(16))) char smem[];
  const unsigned lb = (unsigned)(size_t)smem;
  const int tid = threadIdx.x, lane = tid&63;
  const int wid = __builtin_amdgcn_readfirstlane(tid>>6);
  const int blk = blockIdx.x;
  const int b = blk>>7, p0 = (blk&127)*128;
  const char* xb = (const char*)x + ((size_t)b*C*P + p0)*2;
  char* ob = (char*)out + ((size_t)b*C*P + p0)*2;

  unsigned wv[5];
  #pragma unroll
  for (int i=0;i<5;i++){ unsigned o=i*1024+lane*16; unsigned row=o/640; wv[i]=(lane*16) ^ (((row>>1)&1)<<6); }
  unsigned xv[2];
  #pragma unroll
  for (int e=0;e<2;e++){ unsigned row = (2*wid+e)*4 + (lane>>4); xv[e] = row*(P*2) + (((lane&15) ^ xswz(row))<<4); }

  auto issueW = [&](int ch){
    const char* src = (const char*)(ch<10 ? w_in : w_out) + (ch%10)*20480 + wid*5120 + 2048;
    unsigned dst = lb + WRING + (ch%NSLOT)*20480 + wid*5120 + 2048;
    glds16<-2048>(src, wv[0], dst); glds16<-1024>(src, wv[1], dst); glds16<0>(src, wv[2], dst);
    glds16<1024>(src, wv[3], dst); glds16<2048>(src, wv[4], dst);
  };
  auto issueX = [&](int g){
    const char* src = xb + (size_t)g*32*P*2;
    unsigned dst = lb + XIMG + g*8192 + wid*2048;
    glds16nt<0>(src, xv[0], dst); glds16nt<0>(src, xv[1], dst+1024);
  };
  { int idx = blk>>3;
    #pragma unroll
    for (int j=0;j<4;j++){ int pc = idx*13 + j*4 + wid; if (j==3) pc = idx*13+12; if (pc>399) pc=399;
      const char* src = pc<200 ? (const char*)w_in + pc*1024 : (const char*)w_out + (pc-200)*1024;
      glds16<0>(src, lane*16, lb + WRING + 2*20480 + wid*1024); }
    unsigned long long pcv; asm volatile("s_getpc_b64 %0" : "=s"(pcv));
    glds16<0>((const char*)pcv + (idx&7)*4096 + wid*1024, lane*16, lb + WRING + 2*20480 + 4096 + wid*1024);
  }
  unsigned embraw;
  { unsigned eo = (tid<160?tid:159)*4; asm volatile("global_load_dword %0, %1, %2" : "=v"(embraw) : "v"(eo), "s"(w_emb) : "memory"); }
  issueW(0); issueX(0); issueW(1);
  #pragma unroll
  for (int g=1; g<10; g++) issueX(g);

  unsigned sraw = ((const unsigned*)sigma)[0];
  float s = __uint_as_float(b ? (sraw & 0xffff0000u) : (sraw<<16));
  float denom = rbf(rbf(s*s)+0.25f);
  float c_skip = rbf(0.25f/denom);
  float sq = rbf(sqrtf(denom));
  float c_out = rbf(rbf(s*0.5f)/sq);
  float c_in = rbf(1.0f/sq);
  float c_noise = rbf(0.25f*rbf(logf(s)));
  c_in = fmaxf(c_in, 1e-19f);
  float inv_cin = 1.0f/c_in;
  float tscale = -1.4426950408889634f*c_in;
  float invK = fminf(1.0f/(c_in*c_out), 1e37f);
  asm volatile("s_waitcnt vmcnt(30)" : "+v"(embraw) :: "memory");
  if (tid<160){ *(lds_f32p)(size_t)(lb+BIAS+tid*8) = rbf(c_noise*__uint_as_float(embraw<<16))*inv_cin;
                *(lds_f32p)(size_t)(lb+BIAS+tid*8+4) = rbf(c_noise*__uint_as_float(embraw&0xffff0000u))*inv_cin; }
  waitl0(); bar();

  const int h = lane>>5, blk16=(lane>>4)&1, q=(lane&15)>>2, p=lane&3;
  f32x16 acc[10];
  #pragma unroll
  for (int t=0;t<10;t++)
    #pragma unroll
    for (int g=0;g<4;g++){ f32x4 v = *(lds_f32x4p)(size_t)(lb+BIAS+(32*t+8*g+4*h)*4); acc[t][4*g]=v[0]; acc[t][4*g+1]=v[1]; acc[t][4*g+2]=v[2]; acc[t][4*g+3]=v[3]; }

  const unsigned wb = (q>>1)&1;
  const unsigned wrow = lb + WRING + (4*h+q)*640 + 32*blk16 + 16*(p>>1) + 8*(p&1);
  const unsigned wE = wrow + 64*wb, wO = wrow + 64*(1^wb);
  const unsigned xr = 4*h+q;
  const unsigned xA = lb + XIMG + xr*256 + (((4*wid + 2*blk16 + (p&1)) ^ xswz(xr))<<4) + 8*(p>>1);

  #pragma unroll
  for (int ch=0; ch<10; ch++){
    if (ch==0) waitv<23>();
    else if (ch==1) waitv<16+5>();
    else waitv<5>();
    waitl0(); bar();
    issueW(ch+2);
    #pragma unroll
    for (int s2=0;s2<2;s2++){
      bf16x8 xf = frag(xA + (ch*32+s2*16)*256, xA + (ch*32+s2*16+8)*256);
      #pragma unroll
      for (int t=0;t<10;t++){
        unsigned a = ((t&1)?wO:wE) + (ch%NSLOT)*20480 + (s2*16)*640 + 128*(t>>1);
        bf16x8 wf = frag(a, a+8*640);
        acc[t] = MFMA(wf, xf, acc[t]);
      }
    }
  }
  bf16x8 hf[20];
  #pragma unroll
  for (int t=0;t<10;t++){
    #pragma unroll
    for (int e=0;e<2;e++){
      u32x4 pk;
      #pragma unroll
      for (int j=0;j<4;j++){
        float r[2];
        #pragma unroll
        for (int u=0;u<2;u++){
          float a = acc[t][8*e+2*j+u];
          float ex = __builtin_amdgcn_exp2f(a*tscale);
          float dn = __builtin_fmaf(ex, invK, invK);
          r[u] = a*__builtin_amdgcn_rcpf(dn);
        }
        pk[j] = pk2(r[0], r[1]);
      }
      hf[2*t+e] = __builtin_bit_cast(bf16x8, pk);
    }
  }
  bf16x8 sk[2];
  {
    const u16 cs = f2bf(c_skip);
    #pragma unroll
    for (int e=0;e<2;e++){ s16x8 v;
      #pragma unroll
      for (int j=0;j<8;j++) v[j] = ((lane&31) == 16*e+8*(j>>2)+4*h+(j&3)) ? (short)cs : (short)0;
      sk[e]=__builtin_bit_cast(bf16x8,v); }
  }
  #pragma unroll
  for (int t=0;t<10;t++){
    f32x16 z = {};
    bf16x8 x0 = frag(xA + (t*32)*256, xA + (t*32+8)*256);
    bf16x8 x1 = frag(xA + (t*32+16)*256, xA + (t*32+24)*256);
    z = MFMA(x0, sk[0], z);
    acc[t] = MFMA(x1, sk[1], z);
  }
  #pragma unroll
  for (int ch=10; ch<20; ch++){
    if (ch<19) waitv<5>(); else waitv<0>();
    waitl0(); bar();
    if (ch+2<20) issueW(ch+2);
    #pragma unroll
    for (int s2=0;s2<2;s2++){
      #pragma unroll
      for (int t=0;t<10;t++){
        unsigned a = ((t&1)?wO:wE) + (ch%NSLOT)*20480 + (s2*16)*640 + 128*(t>>1);
        bf16x8 wf = frag(a, a+8*640);
        acc[t] = MFMA(hf[(ch-10)*2+s2], wf, acc[t]);
      }
    }
  }
  {
    const unsigned d = lane&31;
    #pragma unroll
    for (int t=0;t<10;t++){
      #pragma unroll
      for (int e=0;e<2;e++){
        u32x4 pk;
        #pragma unroll
        for (int j=0;j<4;j++) pk[j] = pk2(acc[t][8*e+2*j], acc[t][8*e+2*j+1]);
        unsigned a = lb + XIMG + (32*t+d)*256 + (((4*wid + 2*e + h) ^ xswz(d))<<4);
        *(lds_u32x4p)(size_t)a = pk;
      }
    }
    waitl0(); bar();
    #pragma unroll
    for (int g=0; g<10; g++)
      #pragma unroll
      for (int e=0;e<2;e++){
        u32x4 v = *(lds_u32x4p)(size_t)(lb + XIMG + g*8192 + wid*2048 + e*1024 + lane*16);
        asm volatile("global_store_dwordx4 %0, %1, %2 sc1 nt\n\ts_nop 1" :: "v"(xv[e]), "v"(v), "s"(ob + (size_t)g*32*P*2) : "memory");
      }
  }
}

extern "C" void kernel_launch(void* const* d_in, const int* in_sizes, int n_in,
                              void* d_out, int out_size, void* d_ws, size_t ws_size,
                              hipStream_t stream) {
  (void)hipFuncSetAttribute((const void*)edm_kernel, hipFuncAttributeMaxDynamicSharedMemorySize, LDS_BYTES);
  edm_kernel<<<256,256,LDS_BYTES,stream>>>((const u16*)d_in[0],(const u16*)d_in[1],(const u16*)d_in[2],(const u16*)d_in[3],(const u16*)d_in[4],(u16*)d_out);
}
```

```cpp
#include <hip/hip_runtime.h>
#include <stdint.h>

typedef unsigned short u16;
typedef short s16x4 __attribute__((ext_vector_type(4)));
typedef short s16x8 __attribute__((ext_vector_type(8)));
typedef __bf16 bf16x8 __attribute__((ext_vector_type(8)));
typedef __bf16 bf16x2 __attribute__((ext_vector_type(2)));
typedef float f32x16 __attribute__((ext_vector_type(16)));
typedef float f32x4 __attribute__((ext_vector_type(4)));
typedef float f32x2 __attribute__((ext_vector_type(2)));
typedef unsigned u32x4 __attribute__((ext_vector_type(4)));
typedef __attribute__((address_space(3))) s16x4* lds_s16x4p;
typedef __attribute__((address_space(3))) u32x4* lds_u32x4p;
typedef __attribute__((address_space(3))) f32x4* lds_f32x4p;
typedef __attribute__((address_space(3))) float* lds_f32p;

#define DI __device__ __forceinline__
constexpr int C=320, P=16384;
constexpr int XIMG = 0;
constexpr int WRING = 81920;
constexpr int NSLOT = 3;
constexpr int BIAS = WRING + NSLOT*20480;
constexpr int LDS_BYTES = BIAS + 1280;

DI float bf2f(u16 v){ return __uint_as_float(((unsigned)v)<<16); }
DI u16 f2bf(float f){ __bf16 b = (__bf16)f; return __builtin_bit_cast(u16,b); }
DI float rbf(float f){ return bf2f(f2bf(f)); }
DI unsigned pk2(float lo, float hi){ f32x2 v={lo,hi}; bf16x2 r=__builtin_convertvector(v,bf16x2); return __builtin_bit_cast(unsigned,r); }

template<int OFF> DI void glds16(const void* sbase, unsigned voff, unsigned lds){
  asm volatile("s_mov_b32 m0, %2\n\ts_nop 0\n\tglobal_load_lds_dwordx4 %0, %1 offset:%3" :: "v"(voff), "s"(sbase), "s"(lds), "i"(OFF) : "memory");
}
template<int OFF> DI void glds16nt(const void* sbase, unsigned voff, unsigned lds){
  asm volatile("s_mov_b32 m0, %2\n\ts_nop 0\n\tglobal_load_lds_dwordx4 %0, %1 offset:%3 nt" :: "v"(voff), "s"(sbase), "s"(lds), "i"(OFF) : "memory");
}
template<int N> DI void waitv(){ asm volatile("s_waitcnt vmcnt(%0)" :: "i"(N) : "memory"); }
DI void waitl0(){ asm volatile("s_waitcnt lgkmcnt(0)" ::: "memory"); }
DI unsigned long long now(){ unsigned long long t; __builtin_amdgcn_sched_barrier(0); asm volatile("s_memrealtime %0\n\ts_waitcnt lgkmcnt(0)" : "=s"(t) :: "memory"); __builtin_amdgcn_sched_barrier(0); return t; }
DI void bar(){ __builtin_amdgcn_s_barrier(); }
DI s16x4 trr(unsigned a){ return __builtin_amdgcn_ds_read_tr16_b64_v4i16((lds_s16x4p)(size_t)a); }
DI bf16x8 frag(unsigned a0, unsigned a1){ s16x4 lo=trr(a0), hi=trr(a1); return __builtin_bit_cast(bf16x8, __builtin_shufflevector(lo,hi,0,1,2,3,4,5,6,7)); }
#define MFMA(a,b,c) __builtin_amdgcn_mfma_f32_32x32x16_bf16((a),(b),(c),0,0,0)

DI unsigned xswz(unsigned row){ return (((row>>1)&1)<<3) | ((row&1)<<2) | (((row>>2)&1)<<1) | ((row>>1)&1); }

__global__ __launch_bounds__(256,1) void edm_kernel(const u16* __restrict__ x, const u16* __restrict__ sigma,
      const u16* __restrict__ w_in, const u16* __restrict__ w_emb, const u16* __restrict__ w_out, u16* __restrict__ out)
{
  extern __shared__ __attribute__((aligned(16))) char smem[];
  unsigned long long T[6]; T[0]=now();
  const unsigned lb = (unsigned)(size_t)smem;
  const int tid = threadIdx.x, lane = tid&63;
  const int wid = __builtin_amdgcn_readfirstlane(tid>>6);
  const int blk = blockIdx.x;
  const int b = blk>>7, p0 = (blk&127)*128;
  const char* xb = (const char*)x + ((size_t)b*C*P + p0)*2;
  char* ob = (char*)out + ((size_t)b*C*P + p0)*2;

  unsigned wv[5];
  #pragma unroll
  for (int i=0;i<5;i++){ unsigned o=i*1024+lane*16; unsigned row=o/640; wv[i]=(lane*16) ^ (((row>>1)&1)<<6); }
  unsigned xv[2];
  #pragma unroll
  for (int e=0;e<2;e++){ unsigned row = (2*wid+e)*4 + (lane>>4); xv[e] = row*(P*2) + (((lane&15) ^ xswz(row))<<4); }

  auto issueW = [&](int ch){
    const char* src = (const char*)(ch<10 ? w_in : w_out) + (ch%10)*20480 + wid*5120 + 2048;
    unsigned dst = lb + WRING + (ch%NSLOT)*20480 + wid*5120 + 2048;
    glds16<-2048>(src, wv[0], dst); glds16<-1024>(src, wv[1], dst); glds16<0>(src, wv[2], dst);
    glds16<1024>(src, wv[3], dst); glds16<2048>(src, wv[4], dst);
  };
  auto issueX = [&](int g){
    const char* src = xb + (size_t)g*32*P*2;
    unsigned dst = lb + XIMG + g*8192 + wid*2048;
    glds16nt<0>(src, xv[0], dst); glds16nt<0>(src, xv[1], dst+1024);
  };
  { int idx = blk>>3;
    #pragma unroll
    for (int j=0;j<4;j++){ int pc = idx*13 + j*4 + wid; if (j==3) pc = idx*13+12; if (pc>399) pc=399;
      const char* src = pc<200 ? (const char*)w_in + pc*1024 : (const char*)w_out + (pc-200)*1024;
      glds16<0>(src, lane*16, lb + WRING + 2*20480 + wid*1024); }
    unsigned long long pcv; asm volatile("s_getpc_b64 %0" : "=s"(pcv));
    glds16<0>((const char*)pcv + (idx&7)*4096 + wid*1024, lane*16, lb + WRING + 2*20480 + 4096 + wid*1024);
  }
  unsigned embraw;
  { unsigned eo = (tid<160?tid:159)*4; asm volatile("global_load_dword %0, %1, %2" : "=v"(embraw) : "v"(eo), "s"(w_emb) : "memory"); }
  issueW(0); issueX(0); issueW(1);
  #pragma unroll
  for (int g=1; g<10; g++) issueX(g);

  unsigned sraw = ((const unsigned*)sigma)[0];
  float s = __uint_as_float(b ? (sraw & 0xffff0000u) : (sraw<<16));
  float denom = rbf(rbf(s*s)+0.25f);
  float c_skip = rbf(0.25f/denom);
  float sq = rbf(sqrtf(denom));
  float c_out = rbf(rbf(s*0.5f)/sq);
  float c_in = rbf(1.0f/sq);
  float c_noise = rbf(0.25f*rbf(logf(s)));
  c_in = fmaxf(c_in, 1e-19f);
  float inv_cin = 1.0f/c_in;
  float tscale = -1.4426950408889634f*c_in;
  float invK = fminf(1.0f/(c_in*c_out), 1e37f);
  asm volatile("s_waitcnt vmcnt(30)" : "+v"(embraw) :: "memory");
  if (tid<160){ *(lds_f32p)(size_t)(lb+BIAS+tid*8) = rbf(c_noise*__uint_as_float(embraw<<16))*inv_cin;
                *(lds_f32p)(size_t)(lb+BIAS+tid*8+4) = rbf(c_noise*__uint_as_float(embraw&0xffff0000u))*inv_cin; }
  waitl0(); bar();

  const int h = lane>>5, blk16=(lane>>4)&1, q=(lane&15)>>2, p=lane&3;
  f32x16 acc[10];
  #pragma unroll
  for (int t=0;t<10;t++)
    #pragma unroll
    for (int g=0;g<4;g++){ f32x4 v = *(lds_f32x4p)(size_t)(lb+BIAS+(32*t+8*g+4*h)*4); acc[t][4*g]=v[0]; acc[t][4*g+1]=v[1]; acc[t][4*g+2]=v[2]; acc[t][4*g+3]=v[3]; }

  const unsigned wb = (q>>1)&1;
  const unsigned wrow = lb + WRING + (4*h+q)*640 + 32*blk16 + 16*(p>>1) + 8*(p&1);
  const unsigned wE = wrow + 64*wb, wO = wrow + 64*(1^wb);
  const unsigned xr = 4*h+q;
  const unsigned xA = lb + XIMG + xr*256 + (((4*wid + 2*blk16 + (p&1)) ^ xswz(xr))<<4) + 8*(p>>1);

  #pragma unroll
  for (int ch=0; ch<10; ch++){
    if (ch==0) waitv<23>();
    else if (ch==1) waitv<16+5>();
    else waitv<5>();
    waitl0(); bar();
    if (ch==0) T[1]=now();
    if (ch==2) T[5]=now();
    issueW(ch+2);
    #pragma unroll
    for (int s2=0;s2<2;s2++){
      bf16x8 xf = frag(xA + (ch*32+s2*16)*256, xA + (ch*32+s2*16+8)*256);
      #pragma unroll
      for (int t=0;t<10;t++){
        unsigned a = ((t&1)?wO:wE) + (ch%NSLOT)*20480 + (s2*16)*640 + 128*(t>>1);
        bf16x8 wf = frag(a, a+8*640);
        acc[t] = MFMA(wf, xf, acc[t]);
      }
    }
  }
  T[2]=now();
  bf16x8 hf[20];
  #pragma unroll
  for (int t=0;t<10;t++){
    #pragma unroll
    for (int e=0;e<2;e++){
      u32x4 pk;
      #pragma unroll
      for (int j=0;j<4;j++){
        float r[2];
        #pragma unroll
        for (int u=0;u<2;u++){
          float a = acc[t][8*e+2*j+u];
          float ex = __builtin_amdgcn_exp2f(a*tscale);
          float dn = __builtin_fmaf(ex, invK, invK);
          r[u] = a*__builtin_amdgcn_rcpf(dn);
        }
        pk[j] = pk2(r[0], r[1]);
      }
      hf[2*t+e] = __builtin_bit_cast(bf16x8, pk);
    }
  }
  bf16x8 sk[2];
  {
    const u16 cs = f2bf(c_skip);
    #pragma unroll
    for (int e=0;e<2;e++){ s16x8 v;
      #pragma unroll
      for (int j=0;j<8;j++) v[j] = ((lane&31) == 16*e+8*(j>>2)+4*h+(j&3)) ? (short)cs : (short)0;
      sk[e]=__builtin_bit_cast(bf16x8,v); }
  }
  #pragma unroll
  for (int t=0;t<10;t++){
    f32x16 z = {};
    bf16x8 x0 = frag(xA + (t*32)*256, xA + (t*32+8)*256);
    bf16x8 x1 = frag(xA + (t*32+16)*256, xA + (t*32+24)*256);
    z = MFMA(x0, sk[0], z);
    acc[t] = MFMA(x1, sk[1], z);
  }
  #pragma unroll
  for (int ch=10; ch<20; ch++){
    if (ch<19) waitv<5>(); else waitv<0>();
    waitl0(); bar();
    if (ch+2<20) issueW(ch+2);
    #pragma unroll
    for (int s2=0;s2<2;s2++){
      #pragma unroll
      for (int t=0;t<10;t++){
        unsigned a = ((t&1)?wO:wE) + (ch%NSLOT)*20480 + (s2*16)*640 + 128*(t>>1);
        bf16x8 wf = frag(a, a+8*640);
        acc[t] = MFMA(hf[(ch-10)*2+s2], wf, acc[t]);
      }
    }
  }
  T[3]=now();
  {
    const unsigned d = lane&31;
    #pragma unroll
    for (int t=0;t<10;t++){
      #pragma unroll
      for (int e=0;e<2;e++){
        u32x4 pk;
        #pragma unroll
        for (int j=0;j<4;j++) pk[j] = pk2(acc[t][8*e+2*j], acc[t][8*e+2*j+1]);
        unsigned a = lb + XIMG + (32*t+d)*256 + (((4*wid + 2*e + h) ^ xswz(d))<<4);
        *(lds_u32x4p)(size_t)a = pk;
      }
    }
    waitl0(); bar();
    #pragma unroll
    for (int g=0; g<10; g++)
      #pragma unroll
      for (int e=0;e<2;e++){
        u32x4 v = *(lds_u32x4p)(size_t)(lb + XIMG + g*8192 + wid*2048 + e*1024 + lane*16);
        asm volatile("global_store_dwordx4 %0, %1, %2 sc1 nt\n\ts_nop 1" :: "v"(xv[e]), "v"(v), "s"(ob + (size_t)g*32*P*2) : "memory");
      }
  }
  waitv<0>(); T[4]=now();
  { unsigned long long d = (0ull); unsigned long long t0=now(); while(now()-t0 < 2*d) {} }
}

extern "C" void kernel_launch(void* const* d_in, const int* in_sizes, int n_in,
                              void* d_out, int out_size, void* d_ws, size_t ws_size,
                              hipStream_t stream) {
  (void)hipFuncSetAttribute((const void*)edm_kernel, hipFuncAttributeMaxDynamicSharedMemorySize, LDS_BYTES);
  edm_kernel<<<256,256,LDS_BYTES,stream>>>((const u16*)d_in[0],(const u16*)d_in[1],(const u16*)d_in[2],(const u16*)d_in[3],(const u16*)d_in[4],(u16*)d_out);
}
```

```cpp
#include <hip/hip_runtime.h>
#include <stdint.h>

typedef unsigned short u16;
typedef short s16x4 __attribute__((ext_vector_type(4)));
typedef short s16x8 __attribute__((ext_vector_type(8)));
typedef __bf16 bf16x8 __attribute__((ext_vector_type(8)));
typedef __bf16 bf16x2 __attribute__((ext_vector_type(2)));
typedef float f32x16 __attribute__((ext_vector_type(16)));
typedef float f32x4 __attribute__((ext_vector_type(4)));
typedef float f32x2 __attribute__((ext_vector_type(2)));
typedef unsigned u32x4 __attribute__((ext_vector_type(4)));
typedef __attribute__((address_space(3))) s16x4* lds_s16x4p;
typedef __attribute__((address_space(3))) u32x4* lds_u32x4p;
typedef __attribute__((address_space(3))) f32x4* lds_f32x4p;
typedef __attribute__((address_space(3))) float* lds_f32p;

#define DI __device__ __forceinline__
constexpr int C=320, P=16384;
constexpr int XIMG = 0;
constexpr int WRING = 81920;
constexpr int NSLOT = 3;
constexpr int BIAS = WRING + NSLOT*20480;
constexpr int LDS_BYTES = BIAS + 1280;

DI float bf2f(u16 v){ return __uint_as_float(((unsigned)v)<<16); }
DI u16 f2bf(float f){ __bf16 b = (__bf16)f; return __builtin_bit_cast(u16,b); }
DI float rbf(float f){ return bf2f(f2bf(f)); }
DI unsigned pk2(float lo, float hi){ f32x2 v={lo,hi}; bf16x2 r=__builtin_convertvector(v,bf16x2); return __builtin_bit_cast(unsigned,r); }

template<int OFF> DI void glds16(const void* sbase, unsigned voff, unsigned lds){
  asm volatile("s_mov_b32 m0, %2\n\ts_nop 0\n\tglobal_load_lds_dwordx4 %0, %1 offset:%3" :: "v"(voff), "s"(sbase), "s"(lds), "i"(OFF) : "memory");
}
template<int OFF> DI void glds16nt(const void* sbase, unsigned voff, unsigned lds){
  asm volatile("s_mov_b32 m0, %2\n\ts_nop 0\n\tglobal_load_lds_dwordx4 %0, %1 offset:%3 nt" :: "v"(voff), "s"(sbase), "s"(lds), "i"(OFF) : "memory");
}
template<int N> DI void waitv(){ asm volatile("s_waitcnt vmcnt(%0)" :: "i"(N) : "memory"); }
DI void waitl0(){ asm volatile("s_waitcnt lgkmcnt(0)" ::: "memory"); }
DI unsigned long long now(){ unsigned long long t; __builtin_amdgcn_sched_barrier(0); asm volatile("s_memrealtime %0\n\ts_waitcnt lgkmcnt(0)" : "=s"(t) :: "memory"); __builtin_amdgcn_sched_barrier(0); return t; }
DI void bar(){ __builtin_amdgcn_s_barrier(); }
DI s16x4 trr(unsigned a){ return __builtin_amdgcn_ds_read_tr16_b64_v4i16((lds_s16x4p)(size_t)a); }
DI bf16x8 frag(unsigned a0, unsigned a1){ s16x4 lo=trr(a0), hi=trr(a1); return __builtin_bit_cast(bf16x8, __builtin_shufflevector(lo,hi,0,1,2,3,4,5,6,7)); }
#define MFMA(a,b,c) __builtin_amdgcn_mfma_f32_32x32x16_bf16((a),(b),(c),0,0,0)

DI unsigned xswz(unsigned row){ return (((row>>1)&1)<<3) | ((row&1)<<2) | (((row>>2)&1)<<1) | ((row>>1)&1); }

__global__ __launch_bounds__(256,1) void edm_kernel(const u16* __restrict__ x, const u16* __restrict__ sigma,
      const u16* __restrict__ w_in, const u16* __restrict__ w_emb, const u16* __restrict__ w_out, u16* __restrict__ out)
{
  extern __shared__ __attribute__((aligned(16))) char smem[];
  unsigned long long T[12]; T[0]=now();
  const unsigned lb = (unsigned)(size_t)smem;
  const int tid = threadIdx.x, lane = tid&63;
  const int wid = __builtin_amdgcn_readfirstlane(tid>>6);
  const int blk = blockIdx.x;
  const int b = blk>>7, p0 = (blk&127)*128;
  const char* xb = (const char*)x + ((size_t)b*C*P + p0)*2;
  char* ob = (char*)out + ((size_t)b*C*P + p0)*2;

  asm volatile("" :: "s"(x), "s"(w_in), "s"(w_out), "s"(w_emb), "s"(sigma), "s"(out)); T[6]=now();
  unsigned wv[5];
  #pragma unroll
  for (int i=0;i<5;i++){ unsigned o=i*1024+lane*16; unsigned row=o/640; wv[i]=(lane*16) ^ (((row>>1)&1)<<6); }
  unsigned xv[2];
  #pragma unroll
  for (int e=0;e<2;e++){ unsigned row = (2*wid+e)*4 + (lane>>4); xv[e] = row*(P*2) + (((lane&15) ^ xswz(row))<<4); }

  auto issueW = [&](int ch){
    const char* src = (const char*)(ch<10 ? w_in : w_out) + (ch%10)*20480 + wid*5120 + 2048;
    unsigned dst = lb + WRING + (ch%NSLOT)*20480 + wid*5120 + 2048;
    glds16<-2048>(src, wv[0], dst); glds16<-1024>(src, wv[1], dst); glds16<0>(src, wv[2], dst);
    glds16<1024>(src, wv[3], dst); glds16<2048>(src, wv[4], dst);
  };
  auto issueX = [&](int g){
    const char* src = xb + (size_t)g*32*P*2;
    unsigned dst = lb + XIMG + g*8192 + wid*2048;
    glds16nt<0>(src, xv[0], dst); glds16nt<0>(src, xv[1], dst+1024);
  };
  { int idx = blk>>3;
    #pragma unroll
    for (int j=0;j<4;j++){ int pc = idx*13 + j*4 + wid; if (j==3) pc = idx*13+12; if (pc>399) pc=399;
      const char* src = pc<200 ? (const char*)w_in + pc*1024 : (const char*)w_out + (pc-200)*1024;
      glds16<0>(src, lane*16, lb + WRING + 2*20480 + wid*1024); }
    unsigned long long pcv; asm volatile("s_getpc_b64 %0" : "=s"(pcv));
    glds16<0>((const char*)pcv + (idx&7)*4096 + wid*1024, lane*16, lb + WRING + 2*20480 + 4096 + wid*1024);
  }
  unsigned embraw;
  { unsigned eo = (tid<160?tid:159)*4; asm volatile("global_load_dword %0, %1, %2" : "=v"(embraw) : "v"(eo), "s"(w_emb) : "memory"); }
  issueW(0); issueX(0); issueW(1);
  #pragma unroll
  for (int g=1; g<10; g++) issueX(g);
  T[7]=now();

  unsigned sraw = ((const unsigned*)sigma)[0];
  float s = __uint_as_float(b ? (sraw & 0xffff0000u) : (sraw<<16));
  float denom = rbf(rbf(s*s)+0.25f);
  float c_skip = rbf(0.25f/denom);
  float sq = rbf(sqrtf(denom));
  float c_out = rbf(rbf(s*0.5f)/sq);
  float c_in = rbf(1.0f/sq);
  float c_noise = rbf(0.25f*rbf(logf(s)));
  c_in = fmaxf(c_in, 1e-19f);
  float inv_cin = 1.0f/c_in;
  float tscale = -1.4426950408889634f*c_in;
  float invK = fminf(1.0f/(c_in*c_out), 1e37f);
  asm volatile("" :: "v"(invK), "v"(tscale), "v"(inv_cin), "v"(c_noise), "v"(c_skip)); T[8]=now();
  asm volatile("s_waitcnt vmcnt(30)" : "+v"(embraw) :: "memory"); T[9]=now();
  if (tid<160){ *(lds_f32p)(size_t)(lb+BIAS+tid*8) = rbf(c_noise*__uint_as_float(embraw<<16))*inv_cin;
                *(lds_f32p)(size_t)(lb+BIAS+tid*8+4) = rbf(c_noise*__uint_as_float(embraw&0xffff0000u))*inv_cin; }
  waitl0(); bar();

  const int h = lane>>5, blk16=(lane>>4)&1, q=(lane&15)>>2, p=lane&3;
  f32x16 acc[10];
  #pragma unroll
  for (int t=0;t<10;t++)
    #pragma unroll
    for (int g=0;g<4;g++){ f32x4 v = *(lds_f32x4p)(size_t)(lb+BIAS+(32*t+8*g+4*h)*4); acc[t][4*g]=v[0]; acc[t][4*g+1]=v[1]; acc[t][4*g+2]=v[2]; acc[t][4*g+3]=v[3]; }

  const unsigned wb = (q>>1)&1;
  const unsigned wrow = lb + WRING + (4*h+q)*640 + 32*blk16 + 16*(p>>1) + 8*(p&1);
  const unsigned wE = wrow + 64*wb, wO = wrow + 64*(1^wb);
  const unsigned xr = 4*h+q;
  const unsigned xA = lb + XIMG + xr*256 + (((4*wid + 2*blk16 + (p&1)) ^ xswz(xr))<<4) + 8*(p>>1);

  #pragma unroll
  for (int ch=0; ch<10; ch++){
    if (ch==0) waitv<23>();
    else if (ch==1) waitv<16+5>();
    else waitv<5>();
    waitl0(); bar();
    if (ch==0) T[1]=now();
    if (ch==2) T[5]=now();
    issueW(ch+2);
    #pragma unroll
    for (int s2=0;s2<2;s2++){
      bf16x8 xf = frag(xA + (ch*32+s2*16)*256, xA + (ch*32+s2*16+8)*256);
      #pragma unroll
      for (int t=0;t<10;t++){
        unsigned a = ((t&1)?wO:wE) + (ch%NSLOT)*20480 + (s2*16)*640 + 128*(t>>1);
        bf16x8 wf = frag(a, a+8*640);
        acc[t] = MFMA(wf, xf, acc[t]);
      }
    }
  }
  T[2]=now();
  bf16x8 hf[20];
  #pragma unroll
  for (int t=0;t<10;t++){
    #pragma unroll
    for (int e=0;e<2;e++){
      u32x4 pk;
      #pragma unroll
      for (int j=0;j<4;j++){
        float r[2];
        #pragma unroll
        for (int u=0;u<2;u++){
          float a = acc[t][8*e+2*j+u];
          float ex = __builtin_amdgcn_exp2f(a*tscale);
          float dn = __builtin_fmaf(ex, invK, invK);
          r[u] = a*__builtin_amdgcn_rcpf(dn);
        }
        pk[j] = pk2(r[0], r[1]);
      }
      hf[2*t+e] = __builtin_bit_cast(bf16x8, pk);
    }
  }
  bf16x8 sk[2];
  {
    const u16 cs = f2bf(c_skip);
    #pragma unroll
    for (int e=0;e<2;e++){ s16x8 v;
      #pragma unroll
      for (int j=0;j<8;j++) v[j] = ((lane&31) == 16*e+8*(j>>2)+4*h+(j&3)) ? (short)cs : (short)0;
      sk[e]=__builtin_bit_cast(bf16x8,v); }
  }
  #pragma unroll
  for (int t=0;t<10;t++){
    f32x16 z = {};
    bf16x8 x0 = frag(xA + (t*32)*256, xA + (t*32+8)*256);
    bf16x8 x1 = frag(xA + (t*32+16)*256, xA + (t*32+24)*256);
    z = MFMA(x0, sk[0], z);
    acc[t] = MFMA(x1, sk[1], z);
  }
  #pragma unroll
  for (int ch=10; ch<20; ch++){
    if (ch<19) waitv<5>(); else waitv<0>();
    waitl0(); bar();
    if (ch+2<20) issueW(ch+2);
    #pragma unroll
    for (int s2=0;s2<2;s2++){
      #pragma unroll
      for (int t=0;t<10;t++){
        unsigned a = ((t&1)?wO:wE) + (ch%NSLOT)*20480 + (s2*16)*640 + 128*(t>>1);
        bf16x8 wf = frag(a, a+8*640);
        acc[t] = MFMA(hf[(ch-10)*2+s2], wf, acc[t]);
      }
    }
  }
  T[3]=now();
  {
    const unsigned d = lane&31;
    #pragma unroll
    for (int t=0;t<10;t++){
      #pragma unroll
      for (int e=0;e<2;e++){
        u32x4 pk;
        #pragma unroll
        for (int j=0;j<4;j++) pk[j] = pk2(acc[t][8*e+2*j], acc[t][8*e+2*j+1]);
        unsigned a = lb + XIMG + (32*t+d)*256 + (((4*wid + 2*e + h) ^ xswz(d))<<4);
        *(lds_u32x4p)(size_t)a = pk;
      }
    }
    waitl0(); bar();
    #pragma unroll
    for (int g=0; g<10; g++)
      #pragma unroll
      for (int e=0;e<2;e++){
        u32x4 v = *(lds_u32x4p)(size_t)(lb + XIMG + g*8192 + wid*2048 + e*1024 + lane*16);
        asm volatile("global_store_dwordx4 %0, %1, %2 sc1 nt\n\ts_nop 1" :: "v"(xv[e]), "v"(v), "s"(ob + (size_t)g*32*P*2) : "memory");
      }
  }
  waitv<0>(); T[4]=now();
  { unsigned long long d = (T[9]-T[8]); unsigned long long t0=now(); while(now()-t0 < 4*d) {} }
}

extern "C" void kernel_launch(void* const* d_in, const int* in_sizes, int n_in,
                              void* d_out, int out_size, void* d_ws, size_t ws_size,
                              hipStream_t stream) {
  (void)hipFuncSetAttribute((const void*)edm_kernel, hipFuncAttributeMaxDynamicSharedMemorySize, LDS_BYTES);
  edm_kernel<<<256,256,LDS_BYTES,stream>>>((const u16*)d_in[0],(const u16*)d_in[1],(const u16*)d_in[2],(const u16*)d_in[3],(const u16*)d_in[4],(u16*)d_out);
}
```

```cpp
#include <hip/hip_runtime.h>
#include <stdint.h>

typedef unsigned short u16;
typedef short s16x4 __attribute__((ext_vector_type(4)));
typedef short s16x8 __attribute__((ext_vector_type(8)));
typedef __bf16 bf16x8 __attribute__((ext_vector_type(8)));
typedef __bf16 bf16x2 __attribute__((ext_vector_type(2)));
typedef float f32x16 __attribute__((ext_vector_type(16)));
typedef float f32x4 __attribute__((ext_vector_type(4)));
typedef float f32x2 __attribute__((ext_vector_type(2)));
typedef unsigned u32x4 __attribute__((ext_vector_type(4)));
typedef __attribute__((address_space(3))) s16x4* lds_s16x4p;
typedef __attribute__((address_space(3))) u32x4* lds_u32x4p;
typedef __attribute__((address_space(3))) f32x4* lds_f32x4p;
typedef __attribute__((address_space(3))) float* lds_f32p;

#define DI __device__ __forceinline__
constexpr int C=320, P=16384;
constexpr int XIMG = 0;
constexpr int WRING = 81920;
constexpr int NSLOT = 3;
constexpr int BIAS = WRING + NSLOT*20480;
constexpr int LDS_BYTES = BIAS + 1280;

DI float bf2f(u16 v){ return __uint_as_float(((unsigned)v)<<16); }
DI u16 f2bf(float f){ __bf16 b = (__bf16)f; return __builtin_bit_cast(u16,b); }
DI float rbf(float f){ return bf2f(f2bf(f)); }
DI unsigned pk2(float lo, float hi){ f32x2 v={lo,hi}; bf16x2 r=__builtin_convertvector(v,bf16x2); return __builtin_bit_cast(unsigned,r); }

template<int OFF> DI void glds16(const void* sbase, unsigned voff, unsigned lds){
  asm volatile("s_mov_b32 m0, %2\n\ts_nop 0\n\tglobal_load_lds_dwordx4 %0, %1 offset:%3" :: "v"(voff), "s"(sbase), "s"(lds), "i"(OFF) : "memory");
}
template<int OFF> DI void glds16nt(const void* sbase, unsigned voff, unsigned lds){
  asm volatile("s_mov_b32 m0, %2\n\ts_nop 0\n\tglobal_load_lds_dwordx4 %0, %1 offset:%3 nt" :: "v"(voff), "s"(sbase), "s"(lds), "i"(OFF) : "memory");
}
template<int N> DI void waitv(){ asm volatile("s_waitcnt vmcnt(%0)" :: "i"(N) : "memory"); }
DI void waitl0(){ asm volatile("s_waitcnt lgkmcnt(0)" ::: "memory"); }
DI unsigned long long now(){ unsigned long long t; __builtin_amdgcn_sched_barrier(0); asm volatile("s_memrealtime %0\n\ts_waitcnt lgkmcnt(0)" : "=s"(t) :: "memory"); __builtin_amdgcn_sched_barrier(0); return t; }
DI void bar(){ __builtin_amdgcn_s_barrier(); }
DI s16x4 trr(unsigned a){ return __builtin_amdgcn_ds_read_tr16_b64_v4i16((lds_s16x4p)(size_t)a); }
DI bf16x8 frag(unsigned a0, unsigned a1){ s16x4 lo=trr(a0), hi=trr(a1); return __builtin_bit_cast(bf16x8, __builtin_shufflevector(lo,hi,0,1,2,3,4,5,6,7)); }
#define MFMA(a,b,c) __builtin_amdgcn_mfma_f32_32x32x16_bf16((a),(b),(c),0,0,0)

DI unsigned xswz(unsigned row){ return (((row>>1)&1)<<3) | ((row&1)<<2) | (((row>>2)&1)<<1) | ((row>>1)&1); }

__global__ __launch_bounds__(256,1) void edm_kernel(const u16* __restrict__ x, const u16* __restrict__ sigma,
      const u16* __restrict__ w_in, const u16* __restrict__ w_emb, const u16* __restrict__ w_out, u16* __restrict__ out)
{
  extern __shared__ __attribute__((aligned(16))) char smem[];
  unsigned long long T[12]; T[0]=now();
  const unsigned lb = (unsigned)(size_t)smem;
  const int tid = threadIdx.x, lane = tid&63;
  const int wid = __builtin_amdgcn_readfirstlane(tid>>6);
  const int blk = blockIdx.x;
  const int b = blk>>7, p0 = (blk&127)*128;
  const char* xb = (const char*)x + ((size_t)b*C*P + p0)*2;
  char* ob = (char*)out + ((size_t)b*C*P + p0)*2;

  unsigned wv[5];
  #pragma unroll
  for (int i=0;i<5;i++){ unsigned o=i*1024+lane*16; unsigned row=o/640; wv[i]=(lane*16) ^ (((row>>1)&1)<<6); }
  unsigned xv[2];
  #pragma unroll
  for (int e=0;e<2;e++){ unsigned row = (2*wid+e)*4 + (lane>>4); xv[e] = row*(P*2) + (((lane&15) ^ xswz(row))<<4); }

  auto issueW = [&](int ch){
    const char* src = (const char*)(ch<10 ? w_in : w_out) + (ch%10)*20480 + wid*5120 + 2048;
    unsigned dst = lb + WRING + (ch%NSLOT)*20480 + wid*5120 + 2048;
    glds16<-2048>(src, wv[0], dst); glds16<-1024>(src, wv[1], dst); glds16<0>(src, wv[2], dst);
    glds16<1024>(src, wv[3], dst); glds16<2048>(src, wv[4], dst);
  };
  auto issueX = [&](int g){
    const char* src = xb + (size_t)g*32*P*2;
    unsigned dst = lb + XIMG + g*8192 + wid*2048;
    glds16<0>(src, xv[0], dst); glds16<0>(src, xv[1], dst+1024);
  };
  T[6]=now();
  #pragma unroll
  for (int g=0; g<10; g++) issueX(g);
  T[7]=now();
  waitv<19>(); T[8]=now(); waitv<10>(); T[10]=now(); waitv<0>(); T[9]=now();
  { int idx = blk>>3;
    #pragma unroll
    for (int j=0;j<4;j++){ int pc = idx*13 + j*4 + wid; if (j==3) pc = idx*13+12; if (pc>399) pc=399;
      const char* src = pc<200 ? (const char*)w_in + pc*1024 : (const char*)w_out + (pc-200)*1024;
      glds16<0>(src, lane*16, lb + WRING + 2*20480 + wid*1024); }
    unsigned long long pcv; asm volatile("s_getpc_b64 %0" : "=s"(pcv));
    glds16<0>((const char*)pcv + (idx&7)*4096 + wid*1024, lane*16, lb + WRING + 2*20480 + 4096 + wid*1024);
  }
  unsigned embraw;
  { unsigned eo = (tid<160?tid:159)*4; asm volatile("global_load_dword %0, %1, %2" : "=v"(embraw) : "v"(eo), "s"(w_emb) : "memory"); }
  issueW(0); issueW(1);

  unsigned sraw = ((const unsigned*)sigma)[0];
  float s = __uint_as_float(b ? (sraw & 0xffff0000u) : (sraw<<16));
  float denom = rbf(rbf(s*s)+0.25f);
  float c_skip = rbf(0.25f/denom);
  float sq = rbf(sqrtf(denom));
  float c_out = rbf(rbf(s*0.5f)/sq);
  float c_in = rbf(1.0f/sq);
  float c_noise = rbf(0.25f*rbf(logf(s)));
  c_in = fmaxf(c_in, 1e-19f);
  float inv_cin = 1.0f/c_in;
  float tscale = -1.4426950408889634f*c_in;
  float invK = fminf(1.0f/(c_in*c_out), 1e37f);
  asm volatile("s_waitcnt vmcnt(10)" : "+v"(embraw) :: "memory");
  if (tid<160){ *(lds_f32p)(size_t)(lb+BIAS+tid*8) = rbf(c_noise*__uint_as_float(embraw<<16))*inv_cin;
                *(lds_f32p)(size_t)(lb+BIAS+tid*8+4) = rbf(c_noise*__uint_as_float(embraw&0xffff0000u))*inv_cin; }
  waitl0(); bar();

  const int h = lane>>5, blk16=(lane>>4)&1, q=(lane&15)>>2, p=lane&3;
  f32x16 acc[10];
  #pragma unroll
  for (int t=0;t<10;t++)
    #pragma unroll
    for (int g=0;g<4;g++){ f32x4 v = *(lds_f32x4p)(size_t)(lb+BIAS+(32*t+8*g+4*h)*4); acc[t][4*g]=v[0]; acc[t][4*g+1]=v[1]; acc[t][4*g+2]=v[2]; acc[t][4*g+3]=v[3]; }

  const unsigned wb = (q>>1)&1;
  const unsigned wrow = lb + WRING + (4*h+q)*640 + 32*blk16 + 16*(p>>1) + 8*(p&1);
  const unsigned wE = wrow + 64*wb, wO = wrow + 64*(1^wb);
  const unsigned xr = 4*h+q;
  const unsigned xA = lb + XIMG + xr*256 + (((4*wid + 2*blk16 + (p&1)) ^ xswz(xr))<<4) + 8*(p>>1);

  #pragma unroll
  for (int ch=0; ch<10; ch++){
    if (ch==0) waitv<5>();
    else if (ch==1) waitv<5>();
    else waitv<5>();
    waitl0(); bar();
    if (ch==0) T[1]=now();
    if (ch==2) T[5]=now();
    issueW(ch+2);
    #pragma unroll
    for (int s2=0;s2<2;s2++){
      bf16x8 xf = frag(xA + (ch*32+s2*16)*256, xA + (ch*32+s2*16+8)*256);
      #pragma unroll
      for (int t=0;t<10;t++){
        unsigned a = ((t&1)?wO:wE) + (ch%NSLOT)*20480 + (s2*16)*640 + 128*(t>>1);
        bf16x8 wf = frag(a, a+8*640);
        acc[t] = MFMA(wf, xf, acc[t]);
      }
    }
  }
  T[2]=now();
  bf16x8 hf[20];
  #pragma unroll
  for (int t=0;t<10;t++){
    #pragma unroll
    for (int e=0;e<2;e++){
      u32x4 pk;
      #pragma unroll
      for (int j=0;j<4;j++){
        float r[2];
        #pragma unroll
        for (int u=0;u<2;u++){
          float a = acc[t][8*e+2*j+u];
          float ex = __builtin_amdgcn_exp2f(a*tscale);
          float dn = __builtin_fmaf(ex, invK, invK);
          r[u] = a*__builtin_amdgcn_rcpf(dn);
        }
        pk[j] = pk2(r[0], r[1]);
      }
      hf[2*t+e] = __builtin_bit_cast(bf16x8, pk);
    }
  }
  bf16x8 sk[2];
  {
    const u16 cs = f2bf(c_skip);
    #pragma unroll
    for (int e=0;e<2;e++){ s16x8 v;
      #pragma unroll
      for (int j=0;j<8;j++) v[j] = ((lane&31) == 16*e+8*(j>>2)+4*h+(j&3)) ? (short)cs : (short)0;
      sk[e]=__builtin_bit_cast(bf16x8,v); }
  }
  #pragma unroll
  for (int t=0;t<10;t++){
    f32x16 z = {};
    bf16x8 x0 = frag(xA + (t*32)*256, xA + (t*32+8)*256);
    bf16x8 x1 = frag(xA + (t*32+16)*256, xA + (t*32+24)*256);
    z = MFMA(x0, sk[0], z);
    acc[t] = MFMA(x1, sk[1], z);
  }
  #pragma unroll
  for (int ch=10; ch<20; ch++){
    if (ch<19) waitv<5>(); else waitv<0>();
    waitl0(); bar();
    if (ch+2<20) issueW(ch+2);
    #pragma unroll
    for (int s2=0;s2<2;s2++){
      #pragma unroll
      for (int t=0;t<10;t++){
        unsigned a = ((t&1)?wO:wE) + (ch%NSLOT)*20480 + (s2*16)*640 + 128*(t>>1);
        bf16x8 wf = frag(a, a+8*640);
        acc[t] = MFMA(hf[(ch-10)*2+s2], wf, acc[t]);
      }
    }
  }
  T[3]=now();
  {
    const unsigned d = lane&31;
    #pragma unroll
    for (int t=0;t<10;t++){
      #pragma unroll
      for (int e=0;e<2;e++){
        u32x4 pk;
        #pragma unroll
        for (int j=0;j<4;j++) pk[j] = pk2(acc[t][8*e+2*j], acc[t][8*e+2*j+1]);
        unsigned a = lb + XIMG + (32*t+d)*256 + (((4*wid + 2*e + h) ^ xswz(d))<<4);
        *(lds_u32x4p)(size_t)a = pk;
      }
    }
    waitl0(); bar();
    #pragma unroll
    for (int g=0; g<10; g++)
      #pragma unroll
      for (int e=0;e<2;e++){
        u32x4 v = *(lds_u32x4p)(size_t)(lb + XIMG + g*8192 + wid*2048 + e*1024 + lane*16);
        asm volatile("global_store_dwordx4 %0, %1, %2 sc1 nt\n\ts_nop 1" :: "v"(xv[e]), "v"(v), "s"(ob + (size_t)g*32*P*2) : "memory");
      }
  }
  waitv<0>(); T[4]=now();
  { unsigned long long d = (0ull); unsigned long long t0=now(); while(now()-t0 < 8*d) {} }
}

extern "C" void kernel_launch(void* const* d_in, const int* in_sizes, int n_in,
                              void* d_out, int out_size, void* d_ws, size_t ws_size,
                              hipStream_t stream) {
  (void)hipFuncSetAttribute((const void*)edm_kernel, hipFuncAttributeMaxDynamicSharedMemorySize, LDS_BYTES);
  edm_kernel<<<256,256,LDS_BYTES,stream>>>((const u16*)d_in[0],(const u16*)d_in[1],(const u16*)d_in[2],(const u16*)d_in[3],(const u16*)d_in[4],(u16*)d_out);
}
```

```cpp
#include <hip/hip_runtime.h>
#include <stdint.h>

__global__ __launch_bounds__(256, 1) __attribute__((aligned(4096))) void edm_kernel(const void* x, const void* sigma, const void* w_in, const void* w_emb, const void* w_out, void* out)
{
  asm volatile(
    "s_mov_b64 s[58:59], %[ka]\n\t"
    "s_mov_b32 s63, %[blk]\n\t"
    "s_load_dwordx8 s[64:71], s[58:59], 0x0\n\t"
    "s_load_dwordx4 s[72:75], s[58:59], 0x20\n\t"
    "v_lshrrev_b32 v45, 6, v0\n\t"
    "v_lshrrev_b32 v47, 3, v0\n\t"
    "v_lshrrev_b32 v46, 2, v0\n\t"
    "v_readfirstlane_b32 s62, v45\n\t"
    "s_and_b32 s79, s63, 7\n\t"
    "s_lshl_b32 s79, s79, 5\n\t"
    "s_lshr_b32 s94, s63, 3\n\t"
    "s_or_b32 s79, s79, s94\n\t"
    "s_lshr_b32 s85, s79, 7\n\t"
    "s_mul_i32 s85, s85, 10485760\n\t"
    "v_lshlrev_b32 v13, 15, v47\n\t"
    "v_and_b32 v46, 4, v46\n\t"
    "v_and_b32 v47, 7, v0\n\t"
    "v_xor_b32 v46, v46, v47\n\t"
    "s_and_b32 s93, s79, 127\n\t"
    "s_lshl_b32 s93, s93, 8\n\t"
    "s_add_u32 s85, s85, s93\n\t"
    "s_lshl_b32 s83, s62, 10\n\t"
    "v_lshl_add_u32 v13, v46, 4, v13\n\t"
    "v_lshlrev_b32 v14, 4, v0\n\t"
    "v_and_b32 v44, 63, v0\n\t"
    "s_lshr_b32 s88, s63, 3\n\t"
    "s_getpc_b64 s[96:97]\n\t"
    ".Ledm_pcref%=:\n\t"
    "s_and_b32 s93, s88, 7\n\t"
    "s_lshl_b32 s93, s93, 12\n\t"
    "s_min_u32 s93, s93, .Ledm_codeend%=-.Ledm_pcref%=-4096\n\t"
    "s_add_u32 m0, s83, 12288\n\t"
    "s_add_u32 s96, s96, s93\n\t"
    "s_addc_u32 s97, s97, 0\n\t"
    "global_load_lds_dwordx4 v14, s[96:97]\n\t"
    "v_lshrrev_b32 v60, 4, v0\n\t"
    "v_mul_u32_u24 v61, 640, v60\n\t"
    "v_and_b32 v60, 3, v60\n\t"
    "v_lshlrev_b32 v60, 2, v60\n\t"
    "v_and_b32 v62, 15, v0\n\t"
    "v_xor_b32 v60, v60, v62\n\t"
    "v_lshl_add_u32 v61, v60, 4, v61\n\t"
    "s_mul_i32 s81, s62, 5120\n\t"
    "s_add_u32 s81, s81, 2048\n\t"
    "v_add_u32 v7, 6144, v61\n\t"
    "v_add_u32 v8, 15360, v61\n\t"
    "v_add_u32 v9, 4352, v61\n\t"
    "v_add_u32 v10, 13568, v61\n\t"
    "v_lshrrev_b32 v60, 3, v0\n\t"
    "v_mul_u32_u24 v61, 640, v60\n\t"
    "v_lshl_add_u32 v61, v46, 4, v61\n\t"
    "v_add_u32 v11, 2560, v61\n\t"
    "s_mul_i32 s89, s88, 6400\n\t"
    "v_lshrrev_b32 v14, 1, v0\n\t"
    "v_lshlrev_b32 v14, 6, v14\n\t"
    "v_add_u32 v14, s89, v14\n\t"
    "v_min_u32 v14, 204796, v14\n\t"
    "v_and_b32 v60, 31, v0\n\t"
    "v_lshlrev_b32 v60, 1, v60\n\t"
    "v_lshl_add_u32 v64, v45, 6, v60\n\t"
    "v_lshrrev_b32 v65, 7, v0\n\t"
    "v_lshl_add_u32 v65, v65, 6, v60\n\t"
    "s_waitcnt lgkmcnt(0)\n\t"
    "s_load_dword s95, s[66:67], 0x0\n\t"
    "s_sub_u32 s60, s68, 4096\n\t"
    "s_subb_u32 s61, s69, 0\n\t"
    "global_load_dword v12, v14, s[68:69]\n\t"
    "s_add_u32 m0, s83, 40960\n\t"
    "s_add_u32 s20, s64, s85\n\t"
    "s_addc_u32 s21, s65, 0\n\t"
    "global_load_lds_dwordx4 v13, s[20:21] nt\n\t"
    "s_add_u32 m0, s81, 122880\n\t"
    "s_nop 0\n\t"
    "global_load_lds_dwordx4 v7, s[60:61] offset:-2048\n\t"
    "global_load_lds_dwordx4 v8, s[60:61] offset:-1024\n\t"
    "global_load_lds_dwordx4 v9, s[60:61] offset:0\n\t"
    "global_load_lds_dwordx4 v10, s[60:61] offset:1024\n\t"
    "global_load_lds_dwordx4 v11, s[60:61] offset:2048\n\t"
    "global_load_dword v21, v14, s[72:73]\n\t"
    "global_load_ushort v26, v64, s[70:71]\n\t"
    "global_load_ushort v28, v64, s[70:71] offset:256\n\t"
    "global_load_ushort v30, v65, s[70:71] offset:512\n\t"
    "s_mov_b64 exec, 0\n\t"
    "s_cmp_eq_u32 s62, 0\n\t"
    "s_cbranch_scc1 .Ledm_hop0_%=\n\t"
    "s_cmp_eq_u32 s62, 1\n\t"
    "s_cbranch_scc1 .Ledm_hop1_%=\n\t"
    "s_cmp_eq_u32 s62, 2\n\t"
    "s_cbranch_scc1 .Ledm_hop2_%=\n\t"
    "s_branch .Ledm_hop3_%=\n\t"
    ".Ledm_ret%=:\n\t"
    "s_mov_b64 exec, -1\n\t"
    "s_add_u32 m0, s83, 45056\n\t"
    "s_add_u32 s22, s20, 1048576\n\t"
    "s_addc_u32 s23, s21, 0\n\t"
    "global_load_lds_dwordx4 v13, s[22:23] nt\n\t"
    "s_add_u32 m0, s81, 143360\n\t"
    "s_add_u32 s60, s60, 20480\n\t"
    "s_addc_u32 s61, s61, 0\n\t"
    "global_load_lds_dwordx4 v7, s[60:61] offset:-2048\n\t"
    "global_load_lds_dwordx4 v8, s[60:61] offset:-1024\n\t"
    "global_load_lds_dwordx4 v9, s[60:61] offset:0\n\t"
    "global_load_lds_dwordx4 v10, s[60:61] offset:1024\n\t"
    "global_load_lds_dwordx4 v11, s[60:61] offset:2048\n\t"
    "s_lshr_b32 s82, s62, 1\n\t"
    "s_and_b32 s84, s62, 1\n\t"
    "v_lshrrev_b32 v56, 5, v44\n\t"
    "v_bfe_u32 v57, v44, 4, 1\n\t"
    "v_bfe_u32 v58, v44, 2, 2\n\t"
    "v_and_b32 v59, 3, v44\n\t"
    "v_lshlrev_b32 v60, 3, v59\n\t"
    "v_lshl_add_u32 v60, v57, 5, v60\n\t"
    "v_xor_b32 v61, s62, v58\n\t"
    "v_lshl_add_u32 v61, v61, 6, v60\n\t"
    "v_lshl_add_u32 v61, v58, 8, v61\n\t"
    "v_mul_u32_u24 v62, 5120, v56\n\t"
    "v_add_u32 v61, v61, v62\n\t"
    "v_add_u32 v3, 81920, v61\n\t"
    "v_add_u32 v4, 122880, v61\n\t"
    "v_lshl_add_u32 v62, v56, 2, v58\n\t"
    "v_lshlrev_b32 v62, 7, v62\n\t"
    "v_lshrrev_b32 v63, 1, v58\n\t"
    "v_xor_b32 v61, s82, v63\n\t"
    "v_lshl_add_u32 v61, v61, 6, v60\n\t"
    "v_add_u32 v61, v61, v62\n\t"
    "v_add_u32 v5, 81920, v61\n\t"
    "v_add_u32 v6, 122880, v61\n\t"
    "v_xor_b32 v61, s84, v63\n\t"
    "v_lshl_add_u32 v61, v61, 6, v62\n\t"
    "v_lshl_add_u32 v61, v57, 5, v61\n\t"
    "v_and_b32 v60, 1, v59\n\t"
    "v_lshl_add_u32 v61, v60, 4, v61\n\t"
    "v_lshrrev_b32 v60, 1, v59\n\t"
    "v_lshl_add_u32 v61, v60, 3, v61\n\t"
    "v_add_u32 v1, 40960, v61\n\t"
    "v_xor_b32 v2, 64, v1\n\t"
    "s_add_u32 m0, s83, 49152\n\t"
    "s_add_u32 s24, s22, 1048576\n\t"
    "s_addc_u32 s25, s23, 0\n\t"
    "global_load_lds_dwordx4 v13, s[24:25] nt\n\t"
    "s_add_u32 m0, s81, 81920\n\t"
    "s_add_u32 s60, s60, 20480\n\t"
    "s_addc_u32 s61, s61, 0\n\t"
    "global_load_lds_dwordx4 v7, s[60:61] offset:-2048\n\t"
    "global_load_lds_dwordx4 v8, s[60:61] offset:-1024\n\t"
    "global_load_lds_dwordx4 v9, s[60:61] offset:0\n\t"
    "global_load_lds_dwordx4 v10, s[60:61] offset:1024\n\t"
    "global_load_lds_dwordx4 v11, s[60:61] offset:2048\n\t"
    "s_waitcnt vmcnt(16) lgkmcnt(0)\n\t"
    "s_barrier\n\t"
    "ds_read_b64_tr_b16 a[240:241], v1 offset:0\n\t"
    "ds_read_b64_tr_b16 a[242:243], v1 offset:1024\n\t"
    "ds_read_b64_tr_b16 a[244:245], v2 offset:0\n\t"
    "ds_read_b64_tr_b16 a[246:247], v2 offset:1024\n\t"
    "ds_read_b64_tr_b16 a[0:1], v4 offset:0\n\t"
    "ds_read_b64_tr_b16 a[2:3], v4 offset:10240\n\t"
    "ds_read_b64_tr_b16 a[80:81], v4 offset:2048\n\t"
    "ds_read_b64_tr_b16 a[82:83], v4 offset:12288\n\t"
    "ds_read_b64_tr_b16 a[160:161], v6 offset:4096\n\t"
    "ds_read_b64_tr_b16 a[162:163], v6 offset:9216\n\t"
    "ds_read_b64_tr_b16 a[4:5], v4 offset:1024\n\t"
    "ds_read_b64_tr_b16 a[6:7], v4 offset:11264\n\t"
    "ds_read_b64_tr_b16 a[84:85], v4 offset:3072\n\t"
    "ds_read_b64_tr_b16 a[86:87], v4 offset:13312\n\t"
    "ds_read_b64_tr_b16 a[164:165], v6 offset:14336\n\t"
    "ds_read_b64_tr_b16 a[166:167], v6 offset:19456\n\t"
    "s_waitcnt lgkmcnt(6)\n\t"
    "v_mfma_f32_32x32x16_bf16 v[96:111], a[0:3], a[240:243], 0\n\t"
    "ds_read_b64_tr_b16 a[248:249], v1 offset:2048\n\t"
    "ds_read_b64_tr_b16 a[250:251], v1 offset:3072\n\t"
    "ds_read_b64_tr_b16 a[252:253], v2 offset:2048\n\t"
    "ds_read_b64_tr_b16 a[254:255], v2 offset:3072\n\t"
    "v_mfma_f32_32x32x16_bf16 v[112:127], a[0:3], a[244:247], 0\n\t"
    "s_add_u32 m0, s83, 53248\n\t"
    "s_add_u32 s26, s24, 1048576\n\t"
    "s_addc_u32 s27, s25, 0\n\t"
    "v_mov_b32 v27, 0\n\t"
    "v_mov_b32 v29, 0\n\t"
    "v_mov_b32 v31, 0\n\t"
    "v_mfma_f32_32x32x16_bf16 v[128:143], a[80:83], a[240:243], 0\n\t"
    "global_load_lds_dwordx4 v13, s[26:27] nt\n\t"
    "s_add_u32 m0, s81, 102400\n\t"
    "s_add_u32 s60, s60, 20480\n\t"
    "s_addc_u32 s61, s61, 0\n\t"
    "v_mov_b32 v33, 0\n\t"
    "v_mfma_f32_32x32x16_bf16 v[144:159], a[80:83], a[244:247], 0\n\t"
    "global_load_lds_dwordx4 v7, s[60:61] offset:-2048\n\t"
    "v_mov_b32 v34, 0\n\t"
    "v_mov_b32 v35, 0\n\t"
    "s_lshl_b32 s94, s95, 16\n\t"
    "s_and_b32 s95, s95, 0xffff0000\n\t"
    "s_cmp_ge_u32 s79, 128\n\t"
    "s_cselect_b32 s95, s95, s94\n\t"
    "v_mfma_f32_32x32x16_bf16 v[160:175], a[160:163], a[240:243], 0\n\t"
    "v_mov_b32 v72, s95\n\t"
    "v_mov_b32 v79, 0x3e800000\n\t"
    "v_mul_f32 v73, v72, v72\n\t"
    "v_cvt_pk_bf16_f32 v73, v73, v73\n\t"
    "s_waitcnt vmcnt(8) lgkmcnt(0)\n\t"
    "s_barrier\n\t"
    "v_mfma_f32_32x32x16_bf16 v[96:111], a[4:7], a[248:251], v[96:111]\n\t"
    "global_load_lds_dwordx4 v8, s[60:61] offset:-1024\n\t"
    "ds_read_b64_tr_b16 a[240:241], v1 offset:4096\n\t"
    "ds_read_b64_tr_b16 a[242:243], v1 offset:5120\n\t"
    "ds_read_b64_tr_b16 a[244:245], v2 offset:4096\n\t"
    "ds_read_b64_tr_b16 a[246:247], v2 offset:5120\n\t"
    "v_mfma_f32_32x32x16_bf16 v[112:127], a[4:7], a[252:255], v[112:127]\n\t"
    "ds_read_b64_tr_b16 a[8:9], v4 offset:20480\n\t"
    "ds_read_b64_tr_b16 a[10:11], v4 offset:30720\n\t"
    "ds_read_b64_tr_b16 a[88:89], v4 offset:22528\n\t"
    "v_lshlrev_b32 v73, 16, v73\n\t"
    "v_add_f32 v73, v79, v73\n\t"
    "v_cvt_pk_bf16_f32 v73, v73, v73\n\t"
    "v_mfma_f32_32x32x16_bf16 v[128:143], a[84:87], a[248:251], v[128:143]\n\t"
    "global_load_lds_dwordx4 v9, s[60:61] offset:0\n\t"
    "ds_read_b64_tr_b16 a[90:91], v4 offset:32768\n\t"
    "ds_read_b64_tr_b16 a[168:169], v6 offset:24576\n\t"
    "ds_read_b64_tr_b16 a[170:171], v6 offset:29696\n\t"
    "v_lshlrev_b32 v73, 16, v73\n\t"
    "v_mfma_f32_32x32x16_bf16 v[144:159], a[84:87], a[252:255], v[144:159]\n\t"
    "global_load_lds_dwordx4 v10, s[60:61] offset:1024\n\t"
    "ds_read_b64_tr_b16 a[12:13], v4 offset:21504\n\t"
    "ds_read_b64_tr_b16 a[14:15], v4 offset:31744\n\t"
    "ds_read_b64_tr_b16 a[92:93], v4 offset:23552\n\t"
    "v_sqrt_f32 v82, v73\n\t"
    "v_mfma_f32_32x32x16_bf16 v[160:175], a[164:167], a[248:251], v[160:175]\n\t"
    "ds_read_b64_tr_b16 a[94:95], v4 offset:33792\n\t"
    "ds_read_b64_tr_b16 a[172:173], v6 offset:34816\n\t"
    "ds_read_b64_tr_b16 a[174:175], v6 offset:39936\n\t"
    "v_mov_b32 v87, 0x260\n\t"
    "v_add_u32 v83, -1, v82\n\t"
    "s_waitcnt lgkmcnt(6)\n\t"
    "v_mfma_f32_32x32x16_bf16 v[96:111], a[8:11], a[240:243], v[96:111]\n\t"
    "global_load_lds_dwordx4 v11, s[60:61] offset:2048\n\t"
    "ds_read_b64_tr_b16 a[248:249], v1 offset:6144\n\t"
    "ds_read_b64_tr_b16 a[250:251], v1 offset:7168\n\t"
    "ds_read_b64_tr_b16 a[252:253], v2 offset:6144\n\t"
    "ds_read_b64_tr_b16 a[254:255], v2 offset:7168\n\t"
    "v_mfma_f32_32x32x16_bf16 v[112:127], a[8:11], a[244:247], v[112:127]\n\t"
    "s_add_u32 m0, s83, 57344\n\t"
    "s_add_u32 s28, s26, 1048576\n\t"
    "s_addc_u32 s29, s27, 0\n\t"
    "v_add_u32 v84, 1, v82\n\t"
    "v_fma_f32 v85, -v83, v82, v73\n\t"
    "v_fma_f32 v86, -v84, v82, v73\n\t"
    "v_mfma_f32_32x32x16_bf16 v[128:143], a[88:91], a[240:243], v[128:143]\n\t"
    "global_load_lds_dwordx4 v13, s[28:29] nt\n\t"
    "s_add_u32 m0, s81, 122880\n\t"
    "s_add_u32 s60, s60, 20480\n\t"
    "s_addc_u32 s61, s61, 0\n\t"
    "v_cmp_ge_f32 vcc, 0, v85\n\t"
    "v_mfma_f32_32x32x16_bf16 v[144:159], a[88:91], a[244:247], v[144:159]\n\t"
    "global_load_lds_dwordx4 v7, s[60:61] offset:-2048\n\t"
    "v_cndmask_b32_e32 v82, v82, v83, vcc\n\t"
    "v_cmp_lt_f32 vcc, 0, v86\n\t"
    "s_nop 1\n\t"
    "v_cndmask_b32_e32 v82, v82, v84, vcc\n\t"
    "v_cmp_class_f32 vcc, v73, v87\n\t"
    "s_nop 1\n\t"
    "v_cndmask_b32_e32 v74, v82, v73, vcc\n\t"
    "v_mfma_f32_32x32x16_bf16 v[160:175], a[168:171], a[240:243], v[160:175]\n\t"
    "v_cvt_pk_bf16_f32 v74, v74, v74\n\t"
    "v_lshlrev_b32 v74, 16, v74\n\t"
    "v_div_scale_f32 v82, s[90:91], v74, v74, 1.0\n\t"
    "v_div_scale_f32 v83, vcc, 1.0, v74, 1.0\n\t"
    "v_rcp_f32 v84, v82\n\t"
    "s_waitcnt vmcnt(8) lgkmcnt(0)\n\t"
    "s_barrier\n\t"
    "v_mfma_f32_32x32x16_bf16 v[96:111], a[12:15], a[248:251], v[96:111]\n\t"
    "global_load_lds_dwordx4 v8, s[60:61] offset:-1024\n\t"
    "ds_read_b64_tr_b16 a[240:241], v1 offset:8192\n\t"
    "ds_read_b64_tr_b16 a[242:243], v1 offset:9216\n\t"
    "ds_read_b64_tr_b16 a[244:245], v2 offset:8192\n\t"
    "ds_read_b64_tr_b16 a[246:247], v2 offset:9216\n\t"
    "v_mfma_f32_32x32x16_bf16 v[112:127], a[12:15], a[252:255], v[112:127]\n\t"
    "ds_read_b64_tr_b16 a[16:17], v3 offset:0\n\t"
    "ds_read_b64_tr_b16 a[18:19], v3 offset:10240\n\t"
    "ds_read_b64_tr_b16 a[96:97], v3 offset:2048\n\t"
    "v_fma_f32 v85, -v82, v84, 1.0\n\t"
    "v_fmac_f32 v84, v85, v84\n\t"
    "v_mfma_f32_32x32x16_bf16 v[128:143], a[92:95], a[248:251], v[128:143]\n\t"
    "global_load_lds_dwordx4 v9, s[60:61] offset:0\n\t"
    "ds_read_b64_tr_b16 a[98:99], v3 offset:12288\n\t"
    "ds_read_b64_tr_b16 a[176:177], v5 offset:4096\n\t"
    "ds_read_b64_tr_b16 a[178:179], v5 offset:9216\n\t"
    "v_mul_f32 v85, v83, v84\n\t"
    "v_fma_f32 v86, -v82, v85, v83\n\t"
    "v_mfma_f32_32x32x16_bf16 v[144:159], a[92:95], a[252:255], v[144:159]\n\t"
    "global_load_lds_dwordx4 v10, s[60:61] offset:1024\n\t"
    "ds_read_b64_tr_b16 a[20:21], v3 offset:1024\n\t"
    "ds_read_b64_tr_b16 a[22:23], v3 offset:11264\n\t"
    "ds_read_b64_tr_b16 a[100:101], v3 offset:3072\n\t"
    "v_fmac_f32 v85, v86, v84\n\t"
    "v_mfma_f32_32x32x16_bf16 v[160:175], a[172:175], a[248:251], v[160:175]\n\t"
    "ds_read_b64_tr_b16 a[102:103], v3 offset:13312\n\t"
    "ds_read_b64_tr_b16 a[180:181], v5 offset:14336\n\t"
    "ds_read_b64_tr_b16 a[182:183], v5 offset:19456\n\t"
    "v_fma_f32 v86, -v82, v85, v83\n\t"
    "v_div_fmas_f32 v86, v86, v84, v85\n\t"
    "v_div_fixup_f32 v76, v86, v74, 1.0\n\t"
    "s_waitcnt lgkmcnt(6)\n\t"
    "v_mfma_f32_32x32x16_bf16 v[96:111], a[16:19], a[240:243], v[96:111]\n\t"
    "global_load_lds_dwordx4 v11, s[60:61] offset:2048\n\t"
    "ds_read_b64_tr_b16 a[248:249], v1 offset:10240\n\t"
    "ds_read_b64_tr_b16 a[250:251], v1 offset:11264\n\t"
    "ds_read_b64_tr_b16 a[252:253], v2 offset:10240\n\t"
    "ds_read_b64_tr_b16 a[254:255], v2 offset:11264\n\t"
    "v_mfma_f32_32x32x16_bf16 v[112:127], a[16:19], a[244:247], v[112:127]\n\t"
    "s_add_u32 m0, s83, 61440\n\t"
    "s_add_u32 s30, s28, 1048576\n\t"
    "s_addc_u32 s31, s29, 0\n\t"
    "v_cvt_pk_bf16_f32 v76, v76, v76\n\t"
    "v_lshlrev_b32 v76, 16, v76\n\t"
    "v_mfma_f32_32x32x16_bf16 v[128:143], a[96:99], a[240:243], v[128:143]\n\t"
    "global_load_lds_dwordx4 v13, s[30:31] nt\n\t"
    "s_add_u32 m0, s81, 143360\n\t"
    "s_add_u32 s60, s60, 20480\n\t"
    "s_addc_u32 s61, s61, 0\n\t"
    "v_max_f32 v76, v76, v76\n\t"
    "v_max_f32 v76, 0x1fec1e4a, v76\n\t"
    "v_mfma_f32_32x32x16_bf16 v[144:159], a[96:99], a[244:247], v[144:159]\n\t"
    "global_load_lds_dwordx4 v7, s[60:61] offset:-2048\n\t"
    "v_mov_b32 v82, 0x800000\n\t"
    "v_cmp_gt_f32 vcc, v82, v72\n\t"
    "v_mov_b32 v83, 0\n\t"
    "v_mov_b32 v84, 32\n\t"
    "v_mfma_f32_32x32x16_bf16 v[160:175], a[176:179], a[240:243], v[160:175]\n\t"
    "v_mov_b32 v85, 0x41b17218\n\t"
    "v_cndmask_b32_e32 v84, v83, v84, vcc\n\t"
    "v_cndmask_b32_e32 v85, v83, v85, vcc\n\t"
    "v_ldexp_f32 v82, v72, v84\n\t"
    "v_log_f32 v82, v82\n\t"
    "s_waitcnt vmcnt(8) lgkmcnt(0)\n\t"
    "s_barrier\n\t"
    "v_mfma_f32_32x32x16_bf16 v[96:111], a[20:23], a[248:251], v[96:111]\n\t"
    "global_load_lds_dwordx4 v8, s[60:61] offset:-1024\n\t"
    "ds_read_b64_tr_b16 a[240:241], v1 offset:12288\n\t"
    "ds_read_b64_tr_b16 a[242:243], v1 offset:13312\n\t"
    "ds_read_b64_tr_b16 a[244:245], v2 offset:12288\n\t"
    "ds_read_b64_tr_b16 a[246:247], v2 offset:13312\n\t"
    "v_mfma_f32_32x32x16_bf16 v[112:127], a[20:23], a[252:255], v[112:127]\n\t"
    "ds_read_b64_tr_b16 a[24:25], v3 offset:20480\n\t"
    "ds_read_b64_tr_b16 a[26:27], v3 offset:30720\n\t"
    "ds_read_b64_tr_b16 a[104:105], v3 offset:22528\n\t"
    "v_mov_b32 v86, 0x3f317217\n\t"
    "v_mul_f32 v83, v86, v82\n\t"
    "v_fma_f32 v83, v82, v86, -v83\n\t"
    "v_mfma_f32_32x32x16_bf16 v[128:143], a[100:103], a[248:251], v[128:143]\n\t"
    "global_load_lds_dwordx4 v9, s[60:61] offset:0\n\t"
    "ds_read_b64_tr_b16 a[106:107], v3 offset:32768\n\t"
    "ds_read_b64_tr_b16 a[184:185], v5 offset:24576\n\t"
    "ds_read_b64_tr_b16 a[186:187], v5 offset:29696\n\t"
    "v_fmac_f32 v83, 0x3377d1cf, v82\n\t"
    "v_mfma_f32_32x32x16_bf16 v[144:159], a[100:103], a[252:255], v[144:159]\n\t"
    "global_load_lds_dwordx4 v10, s[60:61] offset:1024\n\t"
    "ds_read_b64_tr_b16 a[28:29], v3 offset:21504\n\t"
    "ds_read_b64_tr_b16 a[30:31], v3 offset:31744\n\t"
    "ds_read_b64_tr_b16 a[108:109], v3 offset:23552\n\t"
    "v_fmac_f32 v83, v86, v82\n\t"
    "v_mfma_f32_32x32x16_bf16 v[160:175], a[180:183], a[248:251], v[160:175]\n\t"
    "ds_read_b64_tr_b16 a[110:111], v3 offset:33792\n\t"
    "ds_read_b64_tr_b16 a[188:189], v5 offset:34816\n\t"
    "ds_read_b64_tr_b16 a[190:191], v5 offset:39936\n\t"
    "v_mov_b32 v84, 0x7f800000\n\t"
    "v_cmp_lt_f32_e64 vcc, |v82|, v84\n\t"
    "s_nop 1\n\t"
    "v_cndmask_b32_e32 v82, v82, v83, vcc\n\t"
    "s_waitcnt lgkmcnt(6)\n\t"
    "v_mfma_f32_32x32x16_bf16 v[96:111], a[24:27], a[240:243], v[96:111]\n\t"
    "global_load_lds_dwordx4 v11, s[60:61] offset:2048\n\t"
    "ds_read_b64_tr_b16 a[248:249], v1 offset:14336\n\t"
    "ds_read_b64_tr_b16 a[250:251], v1 offset:15360\n\t"
    "ds_read_b64_tr_b16 a[252:253], v2 offset:14336\n\t"
    "ds_read_b64_tr_b16 a[254:255], v2 offset:15360\n\t"
    "v_mfma_f32_32x32x16_bf16 v[112:127], a[24:27], a[244:247], v[112:127]\n\t"
    "s_add_u32 m0, s83, 65536\n\t"
    "s_add_u32 s32, s30, 1048576\n\t"
    "s_addc_u32 s33, s31, 0\n\t"
    "v_sub_f32 v78, v82, v85\n\t"
    "v_cvt_pk_bf16_f32 v78, v78, v78\n\t"
    "v_lshlrev_b32 v78, 16, v78\n\t"
    "v_mfma_f32_32x32x16_bf16 v[128:143], a[104:107], a[240:243], v[128:143]\n\t"
    "global_load_lds_dwordx4 v13, s[32:33] nt\n\t"
    "s_add_u32 m0, s81, 81920\n\t"
    "s_add_u32 s60, s60, 20480\n\t"
    "s_addc_u32 s61, s61, 0\n\t"
    "v_mul_f32 v78, v79, v78\n\t"
    "v_mfma_f32_32x32x16_bf16 v[144:159], a[104:107], a[244:247], v[144:159]\n\t"
    "global_load_lds_dwordx4 v7, s[60:61] offset:-2048\n\t"
    "v_cvt_pk_bf16_f32 v78, v78, v78\n\t"
    "v_lshlrev_b32 v78, 16, v78\n\t"
    "v_div_scale_f32 v82, s[90:91], v76, v76, 1.0\n\t"
    "v_div_scale_f32 v83, vcc, 1.0, v76, 1.0\n\t"
    "v_mfma_f32_32x32x16_bf16 v[160:175], a[184:187], a[240:243], v[160:175]\n\t"
    "v_rcp_f32 v84, v82\n\t"
    "s_nop 0\n\t"
    "v_fma_f32 v85, -v82, v84, 1.0\n\t"
    "v_fmac_f32 v84, v85, v84\n\t"
    "v_mul_f32 v85, v83, v84\n\t"
    "v_fma_f32 v86, -v82, v85, v83\n\t"
    "s_waitcnt vmcnt(8) lgkmcnt(0)\n\t"
    "s_barrier\n\t"
    "v_mfma_f32_32x32x16_bf16 v[96:111], a[28:31], a[248:251], v[96:111]\n\t"
    "global_load_lds_dwordx4 v8, s[60:61] offset:-1024\n\t"
    "ds_read_b64_tr_b16 a[240:241], v1 offset:16384\n\t"
    "ds_read_b64_tr_b16 a[242:243], v1 offset:17408\n\t"
    "ds_read_b64_tr_b16 a[244:245], v2 offset:16384\n\t"
    "ds_read_b64_tr_b16 a[246:247], v2 offset:17408\n\t"
    "v_mfma_f32_32x32x16_bf16 v[112:127], a[28:31], a[252:255], v[112:127]\n\t"
    "ds_read_b64_tr_b16 a[32:33], v4 offset:0\n\t"
    "ds_read_b64_tr_b16 a[34:35], v4 offset:10240\n\t"
    "ds_read_b64_tr_b16 a[112:113], v4 offset:2048\n\t"
    "v_fmac_f32 v85, v86, v84\n\t"
    "v_fma_f32 v86, -v82, v85, v83\n\t"
    "v_div_fmas_f32 v86, v86, v84, v85\n\t"
    "v_mfma_f32_32x32x16_bf16 v[128:143], a[108:111], a[248:251], v[128:143]\n\t"
    "global_load_lds_dwordx4 v9, s[60:61] offset:0\n\t"
    "ds_read_b64_tr_b16 a[114:115], v4 offset:12288\n\t"
    "ds_read_b64_tr_b16 a[192:193], v6 offset:4096\n\t"
    "ds_read_b64_tr_b16 a[194:195], v6 offset:9216\n\t"
    "v_div_fixup_f32 v80, v86, v76, 1.0\n\t"
    "v_mfma_f32_32x32x16_bf16 v[144:159], a[108:111], a[252:255], v[144:159]\n\t"
    "global_load_lds_dwordx4 v10, s[60:61] offset:1024\n\t"
    "ds_read_b64_tr_b16 a[36:37], v4 offset:1024\n\t"
    "ds_read_b64_tr_b16 a[38:39], v4 offset:11264\n\t"
    "ds_read_b64_tr_b16 a[116:117], v4 offset:3072\n\t"
    "v_lshlrev_b32 v26, 16, v26\n\t"
    "v_mul_f32 v26, v78, v26\n\t"
    "v_mfma_f32_32x32x16_bf16 v[160:175], a[188:191], a[248:251], v[160:175]\n\t"
    "ds_read_b64_tr_b16 a[118:119], v4 offset:13312\n\t"
    "ds_read_b64_tr_b16 a[196:197], v6 offset:14336\n\t"
    "ds_read_b64_tr_b16 a[198:199], v6 offset:19456\n\t"
    "v_cvt_pk_bf16_f32 v26, v26, v26\n\t"
    "v_lshlrev_b32 v28, 16, v28\n\t"
    "v_mul_f32 v28, v78, v28\n\t"
    "s_waitcnt lgkmcnt(6)\n\t"
    "v_mfma_f32_32x32x16_bf16 v[96:111], a[32:35], a[240:243], v[96:111]\n\t"
    "global_load_lds_dwordx4 v11, s[60:61] offset:2048\n\t"
    "ds_read_b64_tr_b16 a[248:249], v1 offset:18432\n\t"
    "ds_read_b64_tr_b16 a[250:251], v1 offset:19456\n\t"
    "ds_read_b64_tr_b16 a[252:253], v2 offset:18432\n\t"
    "ds_read_b64_tr_b16 a[254:255], v2 offset:19456\n\t"
    "v_mfma_f32_32x32x16_bf16 v[112:127], a[32:35], a[244:247], v[112:127]\n\t"
    "s_add_u32 m0, s83, 69632\n\t"
    "s_add_u32 s34, s32, 1048576\n\t"
    "s_addc_u32 s35, s33, 0\n\t"
    "v_cvt_pk_bf16_f32 v28, v28, v28\n\t"
    "v_lshlrev_b32 v30, 16, v30\n\t"
    "v_mfma_f32_32x32x16_bf16 v[128:143], a[112:115], a[240:243], v[128:143]\n\t"
    "global_load_lds_dwordx4 v13, s[34:35] nt\n\t"
    "s_add_u32 m0, s81, 102400\n\t"
    "s_add_u32 s60, s60, 20480\n\t"
    "s_addc_u32 s61, s61, 0\n\t"
    "v_mul_f32 v30, v78, v30\n\t"
    "v_cvt_pk_bf16_f32 v30, v30, v30\n\t"
    "v_mfma_f32_32x32x16_bf16 v[144:159], a[112:115], a[244:247], v[144:159]\n\t"
    "global_load_lds_dwordx4 v7, s[60:61] offset:-2048\n\t"
    "v_cvt_pk_bf16_f32 v82, v80, v80\n\t"
    "v_lshlrev_b32 v82, 16, v82\n\t"
    "v_sub_f32 v82, v80, v82\n\t"
    "v_cvt_pk_bf16_f32 v82, v80, v82\n\t"
    "v_mfma_f32_32x32x16_bf16 v[160:175], a[192:195], a[240:243], v[160:175]\n\t"
    "v_add_u32 v83, -1, v56\n\t"
    "v_and_b32 v32, v82, v83\n\t"
    "v_div_scale_f32 v82, s[90:91], v73, v73, v79\n\t"
    "v_div_scale_f32 v83, vcc, v79, v73, v79\n\t"
    "v_rcp_f32 v84, v82\n\t"
    "s_waitcnt vmcnt(8) lgkmcnt(0)\n\t"
    "s_barrier\n\t"
    "v_mfma_f32_32x32x16_bf16 v[96:111], a[36:39], a[248:251], v[96:111]\n\t"
    "global_load_lds_dwordx4 v8, s[60:61] offset:-1024\n\t"
    "ds_read_b64_tr_b16 a[240:241], v1 offset:20480\n\t"
    "ds_read_b64_tr_b16 a[242:243], v1 offset:21504\n\t"
    "ds_read_b64_tr_b16 a[244:245], v2 offset:20480\n\t"
    "ds_read_b64_tr_b16 a[246:247], v2 offset:21504\n\t"
    "v_mfma_f32_32x32x16_bf16 v[112:127], a[36:39], a[252:255], v[112:127]\n\t"
    "ds_read_b64_tr_b16 a[40:41], v4 offset:20480\n\t"
    "ds_read_b64_tr_b16 a[42:43], v4 offset:30720\n\t"
    "ds_read_b64_tr_b16 a[120:121], v4 offset:22528\n\t"
    "v_fma_f32 v85, -v82, v84, 1.0\n\t"
    "v_fmac_f32 v84, v85, v84\n\t"
    "v_mfma_f32_32x32x16_bf16 v[128:143], a[116:119], a[248:251], v[128:143]\n\t"
    "global_load_lds_dwordx4 v9, s[60:61] offset:0\n\t"
    "ds_read_b64_tr_b16 a[122:123], v4 offset:32768\n\t"
    "ds_read_b64_tr_b16 a[200:201], v6 offset:24576\n\t"
    "ds_read_b64_tr_b16 a[202:203], v6 offset:29696\n\t"
    "v_mul_f32 v85, v83, v84\n\t"
    "v_fma_f32 v86, -v82, v85, v83\n\t"
    "v_mfma_f32_32x32x16_bf16 v[144:159], a[116:119], a[252:255], v[144:159]\n\t"
    "global_load_lds_dwordx4 v10, s[60:61] offset:1024\n\t"
    "ds_read_b64_tr_b16 a[44:45], v4 offset:21504\n\t"
    "ds_read_b64_tr_b16 a[46:47], v4 offset:31744\n\t"
    "ds_read_b64_tr_b16 a[124:125], v4 offset:23552\n\t"
    "v_fmac_f32 v85, v86, v84\n\t"
    "v_mfma_f32_32x32x16_bf16 v[160:175], a[196:199], a[248:251], v[160:175]\n\t"
    "ds_read_b64_tr_b16 a[126:127], v4 offset:33792\n\t"
    "ds_read_b64_tr_b16 a[204:205], v6 offset:34816\n\t"
    "ds_read_b64_tr_b16 a[206:207], v6 offset:39936\n\t"
    "v_fma_f32 v86, -v82, v85, v83\n\t"
    "v_div_fmas_f32 v86, v86, v84, v85\n\t"
    "v_div_fixup_f32 v77, v86, v73, v79\n\t"
    "s_waitcnt lgkmcnt(6)\n\t"
    "v_mfma_f32_32x32x16_bf16 v[96:111], a[40:43], a[240:243], v[96:111]\n\t"
    "global_load_lds_dwordx4 v11, s[60:61] offset:2048\n\t"
    "ds_read_b64_tr_b16 a[248:249], v1 offset:22528\n\t"
    "ds_read_b64_tr_b16 a[250:251], v1 offset:23552\n\t"
    "ds_read_b64_tr_b16 a[252:253], v2 offset:22528\n\t"
    "ds_read_b64_tr_b16 a[254:255], v2 offset:23552\n\t"
    "v_mfma_f32_32x32x16_bf16 v[112:127], a[40:43], a[244:247], v[112:127]\n\t"
    "s_add_u32 m0, s83, 73728\n\t"
    "s_add_u32 s36, s34, 1048576\n\t"
    "s_addc_u32 s37, s35, 0\n\t"
    "v_cvt_pk_bf16_f32 v77, v77, v77\n\t"
    "v_mul_f32 v75, 0.5, v72\n\t"
    "v_cvt_pk_bf16_f32 v75, v75, v75\n\t"
    "v_mfma_f32_32x32x16_bf16 v[128:143], a[120:123], a[240:243], v[128:143]\n\t"
    "global_load_lds_dwordx4 v13, s[36:37] nt\n\t"
    "s_add_u32 m0, s81, 122880\n\t"
    "s_add_u32 s60, s60, 20480\n\t"
    "s_addc_u32 s61, s61, 0\n\t"
    "v_lshlrev_b32 v75, 16, v75\n\t"
    "v_mfma_f32_32x32x16_bf16 v[96:111], v[26:29], v[32:35], v[96:111]\n\t"
    "v_mfma_f32_32x32x16_bf16 v[144:159], a[120:123], a[244:247], v[144:159]\n\t"
    "global_load_lds_dwordx4 v7, s[60:61] offset:-2048\n\t"
    "v_div_scale_f32 v82, s[90:91], v74, v74, v75\n\t"
    "v_div_scale_f32 v83, vcc, v75, v74, v75\n\t"
    "v_rcp_f32 v84, v82\n\t"
    "v_mfma_f32_32x32x16_bf16 v[160:175], a[200:203], a[240:243], v[160:175]\n\t"
    "v_fma_f32 v85, -v82, v84, 1.0\n\t"
    "v_fmac_f32 v84, v85, v84\n\t"
    "v_mul_f32 v85, v83, v84\n\t"
    "v_fma_f32 v86, -v82, v85, v83\n\t"
    "v_fmac_f32 v85, v86, v84\n\t"
    "v_fma_f32 v86, -v82, v85, v83\n\t"
    "s_waitcnt vmcnt(8) lgkmcnt(0)\n\t"
    "s_barrier\n\t"
    "v_mfma_f32_32x32x16_bf16 v[96:111], a[44:47], a[248:251], v[96:111]\n\t"
    "global_load_lds_dwordx4 v8, s[60:61] offset:-1024\n\t"
    "ds_read_b64_tr_b16 a[240:241], v1 offset:24576\n\t"
    "ds_read_b64_tr_b16 a[242:243], v1 offset:25600\n\t"
    "ds_read_b64_tr_b16 a[244:245], v2 offset:24576\n\t"
    "ds_read_b64_tr_b16 a[246:247], v2 offset:25600\n\t"
    "v_mfma_f32_32x32x16_bf16 v[112:127], a[44:47], a[252:255], v[112:127]\n\t"
    "ds_read_b64_tr_b16 a[48:49], v3 offset:0\n\t"
    "ds_read_b64_tr_b16 a[50:51], v3 offset:10240\n\t"
    "ds_read_b64_tr_b16 a[128:129], v3 offset:2048\n\t"
    "v_div_fmas_f32 v86, v86, v84, v85\n\t"
    "v_div_fixup_f32 v75, v86, v74, v75\n\t"
    "v_cvt_pk_bf16_f32 v75, v75, v75\n\t"
    "v_mfma_f32_32x32x16_bf16 v[128:143], a[124:127], a[248:251], v[128:143]\n\t"
    "global_load_lds_dwordx4 v9, s[60:61] offset:0\n\t"
    "ds_read_b64_tr_b16 a[130:131], v3 offset:12288\n\t"
    "ds_read_b64_tr_b16 a[208:209], v5 offset:4096\n\t"
    "ds_read_b64_tr_b16 a[210:211], v5 offset:9216\n\t"
    "v_lshlrev_b32 v75, 16, v75\n\t"
    "v_mfma_f32_32x32x16_bf16 v[144:159], a[124:127], a[252:255], v[144:159]\n\t"
    "global_load_lds_dwordx4 v10, s[60:61] offset:1024\n\t"
    "ds_read_b64_tr_b16 a[52:53], v3 offset:1024\n\t"
    "ds_read_b64_tr_b16 a[54:55], v3 offset:11264\n\t"
    "ds_read_b64_tr_b16 a[132:133], v3 offset:3072\n\t"
    "v_mul_f32 v75, v76, v75\n\t"
    "v_div_scale_f32 v82, s[90:91], v75, v75, 1.0\n\t"
    "v_mfma_f32_32x32x16_bf16 v[112:127], v[26:29], v[32:35], v[112:127]\n\t"
    "v_mfma_f32_32x32x16_bf16 v[160:175], a[204:207], a[248:251], v[160:175]\n\t"
    "ds_read_b64_tr_b16 a[134:135], v3 offset:13312\n\t"
    "ds_read_b64_tr_b16 a[212:213], v5 offset:14336\n\t"
    "ds_read_b64_tr_b16 a[214:215], v5 offset:19456\n\t"
    "v_div_scale_f32 v83, vcc, 1.0, v75, 1.0\n\t"
    "v_rcp_f32 v84, v82\n\t"
    "s_waitcnt lgkmcnt(6)\n\t"
    "v_mfma_f32_32x32x16_bf16 v[96:111], a[48:51], a[240:243], v[96:111]\n\t"
    "global_load_lds_dwordx4 v11, s[60:61] offset:2048\n\t"
    "ds_read_b64_tr_b16 a[248:249], v1 offset:26624\n\t"
    "ds_read_b64_tr_b16 a[250:251], v1 offset:27648\n\t"
    "ds_read_b64_tr_b16 a[252:253], v2 offset:26624\n\t"
    "ds_read_b64_tr_b16 a[254:255], v2 offset:27648\n\t"
    "v_mfma_f32_32x32x16_bf16 v[112:127], a[48:51], a[244:247], v[112:127]\n\t"
    "s_add_u32 m0, s83, 77824\n\t"
    "s_add_u32 s38, s36, 1048576\n\t"
    "s_addc_u32 s39, s37, 0\n\t"
    "v_fma_f32 v85, -v82, v84, 1.0\n\t"
    "v_fmac_f32 v84, v85, v84\n\t"
    "v_mfma_f32_32x32x16_bf16 v[128:143], a[128:131], a[240:243], v[128:143]\n\t"
    "global_load_lds_dwordx4 v13, s[38:39] nt\n\t"
    "s_add_u32 m0, s81, 143360\n\t"
    "s_add_u32 s60, s60, 20480\n\t"
    "s_addc_u32 s61, s61, 0\n\t"
    "v_mul_f32 v85, v83, v84\n\t"
    "v_mfma_f32_32x32x16_bf16 v[144:159], a[128:131], a[244:247], v[144:159]\n\t"
    "global_load_lds_dwordx4 v7, s[60:61] offset:-2048\n\t"
    "v_fma_f32 v86, -v82, v85, v83\n\t"
    "v_fmac_f32 v85, v86, v84\n\t"
    "v_fma_f32 v86, -v82, v85, v83\n\t"
    "v_div_fmas_f32 v86, v86, v84, v85\n\t"
    "v_div_fixup_f32 v75, v86, v75, 1.0\n\t"
    "v_mfma_f32_32x32x16_bf16 v[160:175], a[208:211], a[240:243], v[160:175]\n\t"
    "v_min_f32 v75, 0x7cf0bdc2, v75\n\t"
    "v_mul_f32 v76, 0xbfb8aa3b, v76\n\t"
    "v_readfirstlane_b32 s80, v77\n\t"
    "v_readfirstlane_b32 s77, v75\n\t"
    "v_readfirstlane_b32 s76, v76\n\t"
    "v_mfma_f32_32x32x16_bf16 v[128:143], v[28:31], v[32:35], v[128:143]\n\t"
    "s_waitcnt vmcnt(8) lgkmcnt(0)\n\t"
    "s_barrier\n\t"
    "v_mfma_f32_32x32x16_bf16 v[96:111], a[52:55], a[248:251], v[96:111]\n\t"
    "global_load_lds_dwordx4 v8, s[60:61] offset:-1024\n\t"
    "ds_read_b64_tr_b16 a[240:241], v1 offset:28672\n\t"
    "ds_read_b64_tr_b16 a[242:243], v1 offset:29696\n\t"
    ".Ledm_hop0_%=:\n\t"
    "s_cbranch_execz .Ledm_ret%=\n\t"
    "ds_read_b64_tr_b16 a[244:245], v2 offset:28672\n\t"
    "ds_read_b64_tr_b16 a[246:247], v2 offset:29696\n\t"
    "v_mfma_f32_32x32x16_bf16 v[112:127], a[52:55], a[252:255], v[112:127]\n\t"
    "ds_read_b64_tr_b16 a[56:57], v3 offset:20480\n\t"
    "ds_read_b64_tr_b16 a[58:59], v3 offset:30720\n\t"
    "ds_read_b64_tr_b16 a[136:137], v3 offset:22528\n\t"
    "s_and_b32 s53, s80, 0xffff\n\t"
    "v_and_b32 v88, 31, v44\n\t"
    "v_lshlrev_b32 v89, 2, v56\n\t"
    "v_mfma_f32_32x32x16_bf16 v[128:143], a[132:135], a[248:251], v[128:143]\n\t"
    "global_load_lds_dwordx4 v9, s[60:61] offset:0\n\t"
    "ds_read_b64_tr_b16 a[138:139], v3 offset:32768\n\t"
    "ds_read_b64_tr_b16 a[216:217], v5 offset:24576\n\t"
    "ds_read_b64_tr_b16 a[218:219], v5 offset:29696\n\t"
    "v_sub_u32 v88, v88, v89\n\t"
    "v_and_b32 v89, 1, v88\n\t"
    "v_mfma_f32_32x32x16_bf16 v[144:159], a[132:135], a[252:255], v[144:159]\n\t"
    "global_load_lds_dwordx4 v10, s[60:61] offset:1024\n\t"
    "ds_read_b64_tr_b16 a[60:61], v3 offset:21504\n\t"
    "ds_read_b64_tr_b16 a[62:63], v3 offset:31744\n\t"
    "ds_read_b64_tr_b16 a[140:141], v3 offset:23552\n\t"
    "v_lshlrev_b32 v89, 4, v89\n\t"
    "v_mfma_f32_32x32x16_bf16 v[160:175], a[212:215], a[248:251], v[160:175]\n\t"
    "ds_read_b64_tr_b16 a[142:143], v3 offset:33792\n\t"
    "ds_read_b64_tr_b16 a[220:221], v5 offset:34816\n\t"
    "ds_read_b64_tr_b16 a[222:223], v5 offset:39936\n\t"
    "v_lshlrev_b32_e64 v89, v89, s53\n\t"
    "v_ashrrev_i32 v88, 1, v88\n\t"
    "v_cmp_eq_u32_e64 s[54:55], 0, v88\n\t"
    "s_waitcnt lgkmcnt(6)\n\t"
    "v_mfma_f32_32x32x16_bf16 v[96:111], a[56:59], a[240:243], v[96:111]\n\t"
    "global_load_lds_dwordx4 v11, s[60:61] offset:2048\n\t"
    "ds_read_b64_tr_b16 a[248:249], v1 offset:30720\n\t"
    "ds_read_b64_tr_b16 a[250:251], v1 offset:31744\n\t"
    "ds_read_b64_tr_b16 a[252:253], v2 offset:30720\n\t"
    "ds_read_b64_tr_b16 a[254:255], v2 offset:31744\n\t"
    "v_add_u32 v13, 128, v13\n\t"
    "s_sub_u32 s60, s72, 4096\n\t"
    "s_subb_u32 s61, s73, 0\n\t"
    "v_mfma_f32_32x32x16_bf16 v[144:159], v[28:31], v[32:35], v[144:159]\n\t"
    "v_mfma_f32_32x32x16_bf16 v[112:127], a[56:59], a[244:247], v[112:127]\n\t"
    "s_add_u32 m0, s83, 0\n\t"
    "v_cmp_eq_u32_e64 s[56:57], 1, v88\n\t"
    "v_cmp_eq_u32_e64 s[90:91], 4, v88\n\t"
    "v_cmp_eq_u32_e64 s[92:93], 5, v88\n\t"
    "v_cndmask_b32_e64 v36, 0, v89, s[54:55]\n\t"
    "v_mfma_f32_32x32x16_bf16 v[128:143], a[136:139], a[240:243], v[128:143]\n\t"
    "global_load_lds_dwordx4 v13, s[20:21] nt\n\t"
    "v_cndmask_b32_e64 v37, 0, v89, s[56:57]\n\t"
    "v_cndmask_b32_e64 v38, 0, v89, s[90:91]\n\t"
    "v_cndmask_b32_e64 v39, 0, v89, s[92:93]\n\t"
    "v_cmp_eq_u32_e64 s[54:55], 8, v88\n\t"
    "v_cmp_eq_u32_e64 s[56:57], 9, v88\n\t"
    "v_mfma_f32_32x32x16_bf16 v[144:159], a[136:139], a[244:247], v[144:159]\n\t"
    "s_add_u32 m0, s83, 4096\n\t"
    "v_cmp_eq_u32_e64 s[90:91], 12, v88\n\t"
    "v_cmp_eq_u32_e64 s[92:93], 13, v88\n\t"
    "v_cndmask_b32_e64 v40, 0, v89, s[54:55]\n\t"
    "v_cndmask_b32_e64 v41, 0, v89, s[56:57]\n\t"
    "v_mfma_f32_32x32x16_bf16 v[160:175], a[216:219], a[240:243], v[160:175]\n\t"
    "global_load_lds_dwordx4 v13, s[22:23] nt\n\t"
    "v_cndmask_b32_e64 v42, 0, v89, s[90:91]\n\t"
    "v_cndmask_b32_e64 v43, 0, v89, s[92:93]\n\t"
    "s_lshl_b32 s86, s62, 12\n\t"
    "s_lshl_b32 s87, s82, 12\n\t"
    "s_add_u32 s87, s87, 32768\n\t"
    "s_waitcnt vmcnt(8) lgkmcnt(0)\n\t"
    "s_barrier\n\t"
    "v_mfma_f32_32x32x16_bf16 v[96:111], a[60:63], a[248:251], v[96:111]\n\t"
    "ds_read_b64_tr_b16 a[240:241], v1 offset:32768\n\t"
    "ds_read_b64_tr_b16 a[242:243], v1 offset:33792\n\t"
    "ds_read_b64_tr_b16 a[244:245], v2 offset:32768\n\t"
    "ds_read_b64_tr_b16 a[246:247], v2 offset:33792\n\t"
    "s_add_u32 m0, s83, 8192\n\t"
    "v_mfma_f32_32x32x16_bf16 v[112:127], a[60:63], a[252:255], v[112:127]\n\t"
    "global_load_lds_dwordx4 v13, s[24:25] nt\n\t"
    "ds_read_b64_tr_b16 a[64:65], v4 offset:0\n\t"
    "ds_read_b64_tr_b16 a[66:67], v4 offset:10240\n\t"
    "ds_read_b64_tr_b16 a[144:145], v4 offset:2048\n\t"
    "v_lshlrev_b32 v60, 4, v44\n\t"
    "v_mfma_f32_32x32x16_bf16 v[160:175], v[30:33], v[32:35], v[160:175]\n\t"
    "v_mfma_f32_32x32x16_bf16 v[128:143], a[140:143], a[248:251], v[128:143]\n\t"
    "ds_read_b64_tr_b16 a[146:147], v4 offset:12288\n\t"
    "ds_read_b64_tr_b16 a[224:225], v6 offset:4096\n\t"
    "ds_read_b64_tr_b16 a[226:227], v6 offset:9216\n\t"
    "s_add_u32 m0, s83, 12288\n\t"
    "s_lshl_b32 s52, s84, 10\n\t"
    "s_add_u32 s52, s52, 122880\n\t"
    "v_mfma_f32_32x32x16_bf16 v[176:191], v[26:29], v[32:35], 0\n\t"
    "v_mfma_f32_32x32x16_bf16 v[144:159], a[140:143], a[252:255], v[144:159]\n\t"
    "global_load_lds_dwordx4 v13, s[26:27] nt\n\t"
    "ds_read_b64_tr_b16 a[68:69], v4 offset:1024\n\t"
    "ds_read_b64_tr_b16 a[70:71], v4 offset:11264\n\t"
    "ds_read_b64_tr_b16 a[148:149], v4 offset:3072\n\t"
    "v_add_u32 v16, s52, v60\n\t"
    "v_mfma_f32_32x32x16_bf16 v[160:175], a[220:223], a[248:251], v[160:175]\n\t"
    "ds_read_b64_tr_b16 a[150:151], v4 offset:13312\n\t"
    "ds_read_b64_tr_b16 a[228:229], v6 offset:14336\n\t"
    "ds_read_b64_tr_b16 a[230:231], v6 offset:19456\n\t"
    "s_add_u32 m0, s83, 16384\n\t"
    "v_xor_b32 v17, 1024, v16\n\t"
    "v_add_u32 v18, s86, v16\n\t"
    "s_waitcnt lgkmcnt(6)\n\t"
    "v_mfma_f32_32x32x16_bf16 v[96:111], a[64:67], a[240:243], v[96:111]\n\t"
    "global_load_lds_dwordx4 v13, s[28:29] nt\n\t"
    "ds_read_b64_tr_b16 a[248:249], v1 offset:34816\n\t"
    "ds_read_b64_tr_b16 a[250:251], v1 offset:35840\n\t"
    "ds_read_b64_tr_b16 a[252:253], v2 offset:34816\n\t"
    "ds_read_b64_tr_b16 a[254:255], v2 offset:35840\n\t"
    "v_mfma_f32_32x32x16_bf16 v[112:127], a[64:67], a[244:247], v[112:127]\n\t"
    "s_add_u32 m0, s83, 20480\n\t"
    "v_add_u32 v19, s86, v17\n\t"
    "v_add_u32 v20, s87, v16\n\t"
    "v_and_b32 v60, 15, v44\n\t"
    "v_lshlrev_b32 v60, 15, v60\n\t"
    "v_lshl_add_u32 v60, v57, 5, v60\n\t"
    "v_mfma_f32_32x32x16_bf16 v[128:143], a[144:147], a[240:243], v[128:143]\n\t"
    "global_load_lds_dwordx4 v13, s[30:31] nt\n\t"
    "v_lshl_add_u32 v60, v56, 4, v60\n\t"
    "s_lshl_b32 s52, s84, 6\n\t"
    "v_add_u32 v15, s52, v60\n\t"
    "v_xor_b32 v25, 64, v15\n\t"
    "v_mfma_f32_32x32x16_bf16 v[144:159], a[144:147], a[244:247], v[144:159]\n\t"
    "s_add_u32 s74, s74, s85\n\t"
    "s_addc_u32 s75, s75, 0\n\t"
    "s_lshl_b32 s52, s62, 20\n\t"
    "s_add_u32 s40, s74, s52\n\t"
    "s_addc_u32 s41, s75, 0\n\t"
    "v_mfma_f32_32x32x16_bf16 v[160:175], a[224:227], a[240:243], v[160:175]\n\t"
    "s_add_u32 s42, s40, 524288\n\t"
    "s_addc_u32 s43, s41, 0\n\t"
    "s_add_u32 s44, s40, 4194304\n\t"
    "s_addc_u32 s45, s41, 0\n\t"
    "s_add_u32 s46, s40, 4718592\n\t"
    "s_addc_u32 s47, s41, 0\n\t"
    "v_mfma_f32_32x32x16_bf16 v[208:223], v[28:31], v[32:35], 0\n\t"
    "s_waitcnt vmcnt(6) lgkmcnt(0)\n\t"
    "s_barrier\n\t"
    "v_mfma_f32_32x32x16_bf16 v[96:111], a[68:71], a[248:251], v[96:111]\n\t"
    "ds_read_b64_tr_b16 a[240:241], v1 offset:36864\n\t"
    "ds_read_b64_tr_b16 a[242:243], v1 offset:37888\n\t"
    "ds_read_b64_tr_b16 a[244:245], v2 offset:36864\n\t"
    "ds_read_b64_tr_b16 a[246:247], v2 offset:37888\n\t"
    "s_add_u32 m0, s81, 81920\n\t"
    "v_mfma_f32_32x32x16_bf16 v[112:127], a[68:71], a[252:255], v[112:127]\n\t"
    "global_load_lds_dwordx4 v7, s[60:61] offset:-2048\n\t"
    "ds_read_b64_tr_b16 a[72:73], v4 offset:20480\n\t"
    "ds_read_b64_tr_b16 a[74:75], v4 offset:30720\n\t"
    "ds_read_b64_tr_b16 a[152:153], v4 offset:22528\n\t"
    "s_lshl_b32 s52, s82, 20\n\t"
    "s_add_u32 s52, s52, 8388608\n\t"
    "v_mfma_f32_32x32x16_bf16 v[128:143], a[148:151], a[248:251], v[128:143]\n\t"
    "ds_read_b64_tr_b16 a[154:155], v4 offset:32768\n\t"
    "ds_read_b64_tr_b16 a[232:233], v6 offset:24576\n\t"
    "ds_read_b64_tr_b16 a[234:235], v6 offset:29696\n\t"
    "s_add_u32 s48, s74, s52\n\t"
    "s_addc_u32 s49, s75, 0\n\t"
    "v_mfma_f32_32x32x16_bf16 v[144:159], a[148:151], a[252:255], v[144:159]\n\t"
    "global_load_lds_dwordx4 v8, s[60:61] offset:-1024\n\t"
    "ds_read_b64_tr_b16 a[76:77], v4 offset:21504\n\t"
    "ds_read_b64_tr_b16 a[78:79], v4 offset:31744\n\t"
    "ds_read_b64_tr_b16 a[156:157], v4 offset:23552\n\t"
    "s_add_u32 s50, s48, 524288\n\t"
    "s_addc_u32 s51, s49, 0\n\t"
    "v_mfma_f32_32x32x16_bf16 v[160:175], a[228:231], a[248:251], v[160:175]\n\t"
    "ds_read_b64_tr_b16 a[158:159], v4 offset:33792\n\t"
    "ds_read_b64_tr_b16 a[236:237], v6 offset:34816\n\t"
    "ds_read_b64_tr_b16 a[238:239], v6 offset:39936\n\t"
    "s_waitcnt lgkmcnt(6)\n\t"
    "v_mfma_f32_32x32x16_bf16 v[96:111], a[72:75], a[240:243], v[96:111]\n\t"
    "global_load_lds_dwordx4 v9, s[60:61] offset:0\n\t"
    "ds_read_b64_tr_b16 a[248:249], v1 offset:38912\n\t"
    "ds_read_b64_tr_b16 a[250:251], v1 offset:39936\n\t"
    "ds_read_b64_tr_b16 a[252:253], v2 offset:38912\n\t"
    "ds_read_b64_tr_b16 a[254:255], v2 offset:39936\n\t"
    "v_add_u32 v44, s86, v1\n\t"
    "v_add_u32 v45, s86, v2\n\t"
    "v_add_u32 v46, s87, v1\n\t"
    "v_mfma_f32_32x32x16_bf16 v[112:127], a[72:75], a[244:247], v[112:127]\n\t"
    "ds_read_b64_tr_b16 v[56:57], v44 offset:0\n\t"
    "ds_read_b64_tr_b16 v[58:59], v44 offset:1024\n\t"
    "v_subrev_u32 v4, 81920, v4\n\t"
    "v_subrev_u32 v6, 81920, v6\n\t"
    "v_mfma_f32_32x32x16_bf16 v[240:255], v[30:33], v[32:35], 0\n\t"
    "v_mfma_f32_32x32x16_bf16 v[128:143], a[152:155], a[240:243], v[128:143]\n\t"
    "global_load_lds_dwordx4 v10, s[60:61] offset:1024\n\t"
    "ds_read_b64_tr_b16 v[64:65], v45 offset:0\n\t"
    "ds_read_b64_tr_b16 v[66:67], v45 offset:1024\n\t"
    "v_mfma_f32_32x32x16_bf16 v[144:159], a[152:155], a[244:247], v[144:159]\n\t"
    "ds_read_b64_tr_b16 v[72:73], v44 offset:16384\n\t"
    "ds_read_b64_tr_b16 v[74:75], v44 offset:17408\n\t"
    "v_mfma_f32_32x32x16_bf16 v[160:175], a[232:235], a[240:243], v[160:175]\n\t"
    "global_load_lds_dwordx4 v11, s[60:61] offset:2048\n\t"
    "ds_read_b64_tr_b16 v[80:81], v45 offset:16384\n\t"
    "ds_read_b64_tr_b16 v[82:83], v45 offset:17408\n\t"
    "s_waitcnt lgkmcnt(8)\n\t"
    "s_barrier\n\t"
    "v_mfma_f32_32x32x16_bf16 v[96:111], a[76:79], a[248:251], v[96:111]\n\t"
    "s_add_u32 m0, s81, 102400\n\t"
    "s_add_u32 s60, s60, 20480\n\t"
    "s_addc_u32 s61, s61, 0\n\t"
    "v_mfma_f32_32x32x16_bf16 v[112:127], a[76:79], a[252:255], v[112:127]\n\t"
    "global_load_lds_dwordx4 v7, s[60:61] offset:-2048\n\t"
    "ds_read_b64_tr_b16 v[60:61], v44 offset:2048\n\t"
    "ds_read_b64_tr_b16 v[62:63], v44 offset:3072\n\t"
    "ds_read_b64_tr_b16 v[68:69], v45 offset:2048\n\t"
    "ds_read_b64_tr_b16 v[70:71], v45 offset:3072\n\t"
    "v_mfma_f32_32x32x16_bf16 v[128:143], a[156:159], a[248:251], v[128:143]\n\t"
    "ds_read_b64_tr_b16 v[76:77], v44 offset:18432\n\t"
    "ds_read_b64_tr_b16 v[78:79], v44 offset:19456\n\t"
    "v_mfma_f32_32x32x16_bf16 v[144:159], a[156:159], a[252:255], v[144:159]\n\t"
    "global_load_lds_dwordx4 v8, s[60:61] offset:-1024\n\t"
    "ds_read_b64_tr_b16 v[84:85], v45 offset:18432\n\t"
    "ds_read_b64_tr_b16 v[86:87], v45 offset:19456\n\t"
    "v_mfma_f32_32x32x16_bf16 v[160:175], a[236:239], a[248:251], v[160:175]\n\t"
    "ds_read_b64_tr_b16 v[88:89], v46 offset:0\n\t"
    "ds_read_b64_tr_b16 v[90:91], v46 offset:1024\n\t"
    "ds_read_b64_tr_b16 v[92:93], v46 offset:2048\n\t"
    "ds_read_b64_tr_b16 v[94:95], v46 offset:3072\n\t"
    "v_subrev_u32 v1, 40960, v1\n\t"
    "v_subrev_u32 v2, 40960, v2\n\t"
    "v_pk_mul_f32 v[26:27], v[96:97], s[76:77] op_sel_hi:[1,0]\n\t"
    "v_pk_mul_f32 v[28:29], v[98:99], s[76:77] op_sel_hi:[1,0]\n\t"
    "global_load_lds_dwordx4 v9, s[60:61] offset:0\n\t"
    "v_exp_f32 v26, v26\n\t"
    "v_exp_f32 v27, v27\n\t"
    "v_pk_mul_f32 v[30:31], v[100:101], s[76:77] op_sel_hi:[1,0]\n\t"
    "v_exp_f32 v28, v28\n\t"
    "v_exp_f32 v29, v29\n\t"
    "global_load_lds_dwordx4 v10, s[60:61] offset:1024\n\t"
    "v_pk_fma_f32 v[26:27], v[26:27], s[76:77], s[76:77] op_sel:[0,1,1] op_sel_hi:[1,1,1]\n\t"
    "v_pk_mul_f32 v[32:33], v[102:103], s[76:77] op_sel_hi:[1,0]\n\t"
    "v_exp_f32 v30, v30\n\t"
    "v_exp_f32 v31, v31\n\t"
    "v_pk_fma_f32 v[28:29], v[28:29], s[76:77], s[76:77] op_sel:[0,1,1] op_sel_hi:[1,1,1]\n\t"
    "global_load_lds_dwordx4 v11, s[60:61] offset:2048\n\t"
    "v_rcp_f32 v26, v26\n\t"
    "v_rcp_f32 v27, v27\n\t"
    "v_pk_mul_f32 v[34:35], v[104:105], s[76:77] op_sel_hi:[1,0]\n\t"
    "v_exp_f32 v32, v32\n\t"
    "v_exp_f32 v33, v33\n\t"
    "v_pk_fma_f32 v[30:31], v[30:31], s[76:77], s[76:77] op_sel:[0,1,1] op_sel_hi:[1,1,1]\n\t"
    "v_rcp_f32 v28, v28\n\t"
    "v_rcp_f32 v29, v29\n\t"
    "v_pk_mul_f32 v[26:27], v[96:97], v[26:27]\n\t"
    "v_cvt_pk_bf16_f32 v96, v26, v27\n\t"
    "s_waitcnt vmcnt(15) lgkmcnt(0)\n\t"
    "s_barrier\n\t"
    "ds_read_b64_tr_b16 a[240:241], v1 offset:0\n\t"
    "ds_read_b64_tr_b16 a[242:243], v1 offset:1024\n\t"
    "ds_read_b64_tr_b16 a[244:245], v2 offset:0\n\t"
    "ds_read_b64_tr_b16 a[246:247], v2 offset:1024\n\t"
    "v_pk_mul_f32 v[26:27], v[106:107], s[76:77] op_sel_hi:[1,0]\n\t"
    "v_exp_f32 v34, v34\n\t"
    "s_add_u32 m0, s81, 40960\n\t"
    "s_add_u32 s60, s60, 20480\n\t"
    "s_addc_u32 s61, s61, 0\n\t"
    "global_load_lds_dwordx4 v7, s[60:61] offset:-2048\n\t"
    "v_exp_f32 v35, v35\n\t"
    "v_pk_fma_f32 v[32:33], v[32:33], s[76:77], s[76:77] op_sel:[0,1,1] op_sel_hi:[1,1,1]\n\t"
    "v_rcp_f32 v30, v30\n\t"
    "v_rcp_f32 v31, v31\n\t"
    "v_pk_mul_f32 v[28:29], v[98:99], v[28:29]\n\t"
    "v_cvt_pk_bf16_f32 v97, v28, v29\n\t"
    "v_pk_mul_f32 v[28:29], v[108:109], s[76:77] op_sel_hi:[1,0]\n\t"
    "v_exp_f32 v26, v26\n\t"
    "v_exp_f32 v27, v27\n\t"
    "v_pk_fma_f32 v[34:35], v[34:35], s[76:77], s[76:77] op_sel:[0,1,1] op_sel_hi:[1,1,1]\n\t"
    "global_load_lds_dwordx4 v8, s[60:61] offset:-1024\n\t"
    "v_rcp_f32 v32, v32\n\t"
    "v_rcp_f32 v33, v33\n\t"
    "v_pk_mul_f32 v[30:31], v[100:101], v[30:31]\n\t"
    "v_cvt_pk_bf16_f32 v98, v30, v31\n\t"
    "v_pk_mul_f32 v[30:31], v[110:111], s[76:77] op_sel_hi:[1,0]\n\t"
    "v_exp_f32 v28, v28\n\t"
    "v_exp_f32 v29, v29\n\t"
    "v_pk_fma_f32 v[26:27], v[26:27], s[76:77], s[76:77] op_sel:[0,1,1] op_sel_hi:[1,1,1]\n\t"
    "v_rcp_f32 v34, v34\n\t"
    "v_rcp_f32 v35, v35\n\t"
    "global_load_lds_dwordx4 v9, s[60:61] offset:0\n\t"
    "v_pk_mul_f32 v[32:33], v[102:103], v[32:33]\n\t"
    "v_cvt_pk_bf16_f32 v99, v32, v33\n\t"
    "v_exp_f32 v30, v30\n\t"
    "v_exp_f32 v31, v31\n\t"
    "v_pk_fma_f32 v[28:29], v[28:29], s[76:77], s[76:77] op_sel:[0,1,1] op_sel_hi:[1,1,1]\n\t"
    "v_rcp_f32 v26, v26\n\t"
    "v_rcp_f32 v27, v27\n\t"
    "v_pk_mul_f32 v[34:35], v[104:105], v[34:35]\n\t"
    "v_cvt_pk_bf16_f32 v104, v34, v35\n\t"
    "v_pk_fma_f32 v[30:31], v[30:31], s[76:77], s[76:77] op_sel:[0,1,1] op_sel_hi:[1,1,1]\n\t"
    "v_rcp_f32 v28, v28\n\t"
    "v_rcp_f32 v29, v29\n\t"
    "v_pk_mul_f32 v[26:27], v[106:107], v[26:27]\n\t"
    "v_cvt_pk_bf16_f32 v105, v26, v27\n\t"
    "v_rcp_f32 v30, v30\n\t"
    "v_rcp_f32 v31, v31\n\t"
    "v_pk_mul_f32 v[28:29], v[108:109], v[28:29]\n\t"
    "v_cvt_pk_bf16_f32 v106, v28, v29\n\t"
    "v_pk_mul_f32 v[30:31], v[110:111], v[30:31]\n\t"
    "v_cvt_pk_bf16_f32 v107, v30, v31\n\t"
    "s_waitcnt lgkmcnt(0)\n\t"
    "v_mfma_f32_32x32x16_bf16 v[192:207], a[0:3], a[244:247], v[176:191]\n\t"
    "ds_read_b64_tr_b16 a[248:249], v1 offset:2048\n\t"
    "ds_read_b64_tr_b16 a[250:251], v1 offset:3072\n\t"
    "ds_read_b64_tr_b16 a[252:253], v2 offset:2048\n\t"
    "ds_read_b64_tr_b16 a[254:255], v2 offset:3072\n\t"
    "v_mul_f32 v26, s76, v112\n\t"
    "v_mul_f32 v27, s76, v113\n\t"
    "v_exp_f32 v26, v26\n\t"
    "v_mfma_f32_32x32x16_bf16 v[176:191], a[0:3], a[240:243], v[176:191]\n\t"
    "global_load_lds_dwordx4 v10, s[60:61] offset:1024\n\t"
    "v_mul_f32 v28, s76, v114\n\t"
    "v_exp_f32 v27, v27\n\t"
    "v_fma_f32 v26, v26, s77, s77\n\t"
    "v_mul_f32 v29, s76, v115\n\t"
    "v_exp_f32 v28, v28\n\t"
    "v_mfma_f32_32x32x16_bf16 v[224:239], a[80:83], a[244:247], v[208:223]\n\t"
    "global_load_lds_dwordx4 v11, s[60:61] offset:2048\n\t"
    "s_add_u32 m0, s81, 61440\n\t"
    "s_add_u32 s60, s60, 20480\n\t"
    "s_addc_u32 s61, s61, 0\n\t"
    "v_fma_f32 v27, v27, s77, s77\n\t"
    "v_rcp_f32 v26, v26\n\t"
    "v_mul_f32 v30, s76, v116\n\t"
    "v_mfma_f32_32x32x16_bf16 v[208:223], a[80:83], a[240:243], v[208:223]\n\t"
    "global_load_lds_dwordx4 v7, s[60:61] offset:-2048\n\t"
    "v_exp_f32 v29, v29\n\t"
    "v_fma_f32 v28, v28, s77, s77\n\t"
    "v_rcp_f32 v27, v27\n\t"
    "v_mul_f32 v26, v112, v26\n\t"
    "v_mul_f32 v31, s76, v117\n\t"
    "v_mfma_f32_32x32x16_bf16 v[240:255], a[160:163], a[240:243], v[240:255]\n\t"
    "global_load_lds_dwordx4 v8, s[60:61] offset:-1024\n\t"
    "v_exp_f32 v30, v30\n\t"
    "v_fma_f32 v29, v29, s77, s77\n\t"
    "v_rcp_f32 v28, v28\n\t"
    "v_mul_f32 v27, v113, v27\n\t"
    "v_cvt_pk_bf16_f32 v112, v26, v27\n\t"
    "v_mul_f32 v32, s76, v118\n\t"
    "s_waitcnt vmcnt(20) lgkmcnt(0)\n\t"
    "s_barrier\n\t"
    "v_mfma_f32_32x32x16_bf16 v[176:191], a[4:7], a[248:251], v[176:191]\n\t"
    "ds_read_b64_tr_b16 a[240:241], v1 offset:4096\n\t"
    "ds_read_b64_tr_b16 a[242:243], v1 offset:5120\n\t"
    "ds_read_b64_tr_b16 a[244:245], v2 offset:4096\n\t"
    "ds_read_b64_tr_b16 a[246:247], v2 offset:5120\n\t"
    "v_exp_f32 v31, v31\n\t"
    "v_mfma_f32_32x32x16_bf16 v[192:207], a[4:7], a[252:255], v[192:207]\n\t"
    "global_load_lds_dwordx4 v9, s[60:61] offset:0\n\t"
    "v_fma_f32 v30, v30, s77, s77\n\t"
    "v_rcp_f32 v29, v29\n\t"
    "v_mul_f32 v28, v114, v28\n\t"
    "v_mul_f32 v33, s76, v119\n\t"
    "v_exp_f32 v32, v32\n\t"
    "v_mfma_f32_32x32x16_bf16 v[208:223], a[84:87], a[248:251], v[208:223]\n\t"
    "global_load_lds_dwordx4 v10, s[60:61] offset:1024\n\t"
    "v_fma_f32 v31, v31, s77, s77\n\t"
    "v_rcp_f32 v30, v30\n\t"
    "v_mul_f32 v29, v115, v29\n\t"
    "v_cvt_pk_bf16_f32 v113, v28, v29\n\t"
    "v_mul_f32 v34, s76, v120\n\t"
    "v_exp_f32 v33, v33\n\t"
    "v_mfma_f32_32x32x16_bf16 v[224:239], a[84:87], a[252:255], v[224:239]\n\t"
    "global_load_lds_dwordx4 v11, s[60:61] offset:2048\n\t"
    "s_add_u32 m0, s83, 24576\n\t"
    "v_fma_f32 v32, v32, s77, s77\n\t"
    "v_rcp_f32 v31, v31\n\t"
    "v_mul_f32 v30, v116, v30\n\t"
    "v_mul_f32 v35, s76, v121\n\t"
    "v_mfma_f32_32x32x16_bf16 v[240:255], a[164:167], a[248:251], v[240:255]\n\t"
    "global_load_lds_dwordx4 v13, s[32:33] nt\n\t"
    "v_exp_f32 v34, v34\n\t"
    "v_fma_f32 v33, v33, s77, s77\n\t"
    "v_rcp_f32 v32, v32\n\t"
    "v_mul_f32 v31, v117, v31\n\t"
    "v_cvt_pk_bf16_f32 v114, v30, v31\n\t"
    "v_mul_f32 v26, s76, v122\n\t"
    "s_waitcnt lgkmcnt(0)\n\t"
    "v_mfma_f32_32x32x16_bf16 v[176:191], a[8:11], a[240:243], v[176:191]\n\t"
    "ds_read_b64_tr_b16 a[248:249], v1 offset:6144\n\t"
    "ds_read_b64_tr_b16 a[250:251], v1 offset:7168\n\t"
    "ds_read_b64_tr_b16 a[252:253], v2 offset:6144\n\t"
    "ds_read_b64_tr_b16 a[254:255], v2 offset:7168\n\t"
    "s_add_u32 m0, s83, 28672\n\t"
    "v_exp_f32 v35, v35\n\t"
    "v_fma_f32 v34, v34, s77, s77\n\t"
    "v_mfma_f32_32x32x16_bf16 v[192:207], a[8:11], a[244:247], v[192:207]\n\t"
    "global_load_lds_dwordx4 v13, s[34:35] nt\n\t"
    "s_add_u32 m0, s83, 32768\n\t"
    "v_rcp_f32 v33, v33\n\t"
    "v_mul_f32 v32, v118, v32\n\t"
    "v_mul_f32 v27, s76, v123\n\t"
    "v_exp_f32 v26, v26\n\t"
    "v_mfma_f32_32x32x16_bf16 v[208:223], a[88:91], a[240:243], v[208:223]\n\t"
    "global_load_lds_dwordx4 v13, s[36:37] nt\n\t"
    "s_add_u32 m0, s83, 36864\n\t"
    "v_fma_f32 v35, v35, s77, s77\n\t"
    "v_rcp_f32 v34, v34\n\t"
    "v_mul_f32 v33, v119, v33\n\t"
    "v_cvt_pk_bf16_f32 v115, v32, v33\n\t"
    "v_mfma_f32_32x32x16_bf16 v[224:239], a[88:91], a[244:247], v[224:239]\n\t"
    "global_load_lds_dwordx4 v13, s[38:39] nt\n\t"
    "v_mul_f32 v28, s76, v124\n\t"
    "v_exp_f32 v27, v27\n\t"
    "v_fma_f32 v26, v26, s77, s77\n\t"
    "v_mfma_f32_32x32x16_bf16 v[240:255], a[168:171], a[240:243], v[240:255]\n\t"
    "v_rcp_f32 v35, v35\n\t"
    "v_mul_f32 v34, v120, v34\n\t"
    "v_mul_f32 v29, s76, v125\n\t"
    "v_exp_f32 v28, v28\n\t"
    "v_fma_f32 v27, v27, s77, s77\n\t"
    "v_rcp_f32 v26, v26\n\t"
    "s_waitcnt lgkmcnt(0)\n\t"
    "v_mfma_f32_32x32x16_bf16 v[176:191], a[12:15], a[248:251], v[176:191]\n\t"
    "ds_read_b64_tr_b16 a[240:241], v1 offset:8192\n\t"
    "ds_read_b64_tr_b16 a[242:243], v1 offset:9216\n\t"
    "ds_read_b64_tr_b16 a[244:245], v2 offset:8192\n\t"
    "ds_read_b64_tr_b16 a[246:247], v2 offset:9216\n\t"
    "v_mul_f32 v35, v121, v35\n\t"
    "v_cvt_pk_bf16_f32 v120, v34, v35\n\t"
    "v_mul_f32 v30, s76, v126\n\t"
    "v_exp_f32 v29, v29\n\t"
    "v_mfma_f32_32x32x16_bf16 v[192:207], a[12:15], a[252:255], v[192:207]\n\t"
    "v_fma_f32 v28, v28, s77, s77\n\t"
    "v_rcp_f32 v27, v27\n\t"
    "v_mul_f32 v26, v122, v26\n\t"
    "v_mul_f32 v31, s76, v127\n\t"
    "v_exp_f32 v30, v30\n\t"
    "v_fma_f32 v29, v29, s77, s77\n\t"
    "v_mfma_f32_32x32x16_bf16 v[208:223], a[92:95], a[248:251], v[208:223]\n\t"
    "v_rcp_f32 v28, v28\n\t"
    "v_mul_f32 v27, v123, v27\n\t"
    "v_cvt_pk_bf16_f32 v121, v26, v27\n\t"
    "v_mul_f32 v32, s76, v128\n\t"
    "v_exp_f32 v31, v31\n\t"
    "v_fma_f32 v30, v30, s77, s77\n\t"
    "v_mfma_f32_32x32x16_bf16 v[224:239], a[92:95], a[252:255], v[224:239]\n\t"
    "v_rcp_f32 v29, v29\n\t"
    "v_mul_f32 v28, v124, v28\n\t"
    "v_mul_f32 v33, s76, v129\n\t"
    "v_exp_f32 v32, v32\n\t"
    "v_fma_f32 v31, v31, s77, s77\n\t"
    "v_rcp_f32 v30, v30\n\t"
    "v_mfma_f32_32x32x16_bf16 v[240:255], a[172:175], a[248:251], v[240:255]\n\t"
    "v_mul_f32 v29, v125, v29\n\t"
    "v_cvt_pk_bf16_f32 v122, v28, v29\n\t"
    "v_mul_f32 v34, s76, v130\n\t"
    "v_exp_f32 v33, v33\n\t"
    "v_fma_f32 v32, v32, s77, s77\n\t"
    "v_rcp_f32 v31, v31\n\t"
    "s_waitcnt vmcnt(19) lgkmcnt(0)\n\t"
    "s_barrier\n\t"
    "v_mfma_f32_32x32x16_bf16 v[176:191], a[16:19], a[240:243], v[176:191]\n\t"
    "ds_read_b64_tr_b16 a[248:249], v1 offset:10240\n\t"
    "ds_read_b64_tr_b16 a[250:251], v1 offset:11264\n\t"
    "ds_read_b64_tr_b16 a[252:253], v2 offset:10240\n\t"
    "ds_read_b64_tr_b16 a[254:255], v2 offset:11264\n\t"
    "v_mul_f32 v30, v126, v30\n\t"
    "v_mul_f32 v35, s76, v131\n\t"
    "v_mfma_f32_32x32x16_bf16 v[192:207], a[16:19], a[244:247], v[192:207]\n\t"
    "ds_read_b64_tr_b16 a[0:1], v3 offset:0\n\t"
    "ds_read_b64_tr_b16 a[2:3], v3 offset:10240\n\t"
    "ds_read_b64_tr_b16 a[4:5], v3 offset:1024\n\t"
    "v_exp_f32 v34, v34\n\t"
    "v_fma_f32 v33, v33, s77, s77\n\t"
    "v_rcp_f32 v32, v32\n\t"
    "v_mul_f32 v31, v127, v31\n\t"
    "v_mfma_f32_32x32x16_bf16 v[208:223], a[96:99], a[240:243], v[208:223]\n\t"
    "ds_read_b64_tr_b16 a[6:7], v3 offset:11264\n\t"
    "ds_read_b64_tr_b16 a[160:161], v5 offset:4096\n\t"
    "ds_read_b64_tr_b16 a[162:163], v5 offset:9216\n\t"
    "ds_write_b128 v18, v[96:99] offset:0\n\t"
    "v_cvt_pk_bf16_f32 v123, v30, v31\n\t"
    "v_mfma_f32_32x32x16_bf16 v[224:239], a[96:99], a[244:247], v[224:239]\n\t"
    "ds_read_b64_tr_b16 a[164:165], v5 offset:14336\n\t"
    "ds_read_b64_tr_b16 a[166:167], v5 offset:19456\n\t"
    "ds_read_b64_tr_b16 a[80:81], v3 offset:2048\n\t"
    "v_mul_f32 v26, s76, v132\n\t"
    "v_mfma_f32_32x32x16_bf16 v[240:255], a[176:179], a[240:243], v[240:255]\n\t"
    "ds_read_b64_tr_b16 a[82:83], v3 offset:12288\n\t"
    "ds_read_b64_tr_b16 a[84:85], v3 offset:3072\n\t"
    "ds_read_b64_tr_b16 a[86:87], v3 offset:13312\n\t"
    "ds_write_b128 v18, v[104:107] offset:2048\n\t"
    "v_exp_f32 v35, v35\n\t"
    "s_waitcnt lgkmcnt(14)\n\t"
    "v_mfma_f32_32x32x16_bf16 v[176:191], a[20:23], a[248:251], v[176:191]\n\t"
    "ds_read_b64_tr_b16 a[240:241], v1 offset:12288\n\t"
    "ds_read_b64_tr_b16 a[242:243], v1 offset:13312\n\t"
    "ds_read_b64_tr_b16 a[244:245], v2 offset:12288\n\t"
    "ds_read_b64_tr_b16 a[246:247], v2 offset:13312\n\t"
    "v_fma_f32 v34, v34, s77, s77\n\t"
    "v_rcp_f32 v33, v33\n\t"
    "v_mul_f32 v32, v128, v32\n\t"
    "v_mfma_f32_32x32x16_bf16 v[192:207], a[20:23], a[252:255], v[192:207]\n\t"
    "v_mul_f32 v27, s76, v133\n\t"
    "v_exp_f32 v26, v26\n\t"
    "v_fma_f32 v35, v35, s77, s77\n\t"
    "v_rcp_f32 v34, v34\n\t"
    "v_mul_f32 v33, v129, v33\n\t"
    "v_cvt_pk_bf16_f32 v128, v32, v33\n\t"
    "v_mfma_f32_32x32x16_bf16 v[208:223], a[100:103], a[248:251], v[208:223]\n\t"
    "ds_write_b128 v19, v[112:115] offset:0\n\t"
    "v_mul_f32 v28, s76, v134\n\t"
    "v_exp_f32 v27, v27\n\t"
    "v_fma_f32 v26, v26, s77, s77\n\t"
    "v_rcp_f32 v35, v35\n\t"
    "v_mul_f32 v34, v130, v34\n\t"
    "v_mul_f32 v29, s76, v135\n\t"
    "v_mfma_f32_32x32x16_bf16 v[224:239], a[100:103], a[252:255], v[224:239]\n\t"
    "v_exp_f32 v28, v28\n\t"
    "v_fma_f32 v27, v27, s77, s77\n\t"
    "v_rcp_f32 v26, v26\n\t"
    "v_mul_f32 v35, v131, v35\n\t"
    "v_cvt_pk_bf16_f32 v129, v34, v35\n\t"
    "v_mul_f32 v30, s76, v136\n\t"
    "v_exp_f32 v29, v29\n\t"
    "v_mfma_f32_32x32x16_bf16 v[240:255], a[180:183], a[248:251], v[240:255]\n\t"
    "v_fma_f32 v28, v28, s77, s77\n\t"
    "v_rcp_f32 v27, v27\n\t"
    "v_mul_f32 v26, v132, v26\n\t"
    "v_mul_f32 v31, s76, v137\n\t"
    "v_exp_f32 v30, v30\n\t"
    "v_fma_f32 v29, v29, s77, s77\n\t"
    "s_waitcnt vmcnt(14) lgkmcnt(1)\n\t"
    "s_barrier\n\t"
    "v_mfma_f32_32x32x16_bf16 v[176:191], a[24:27], a[240:243], v[176:191]\n\t"
    "ds_read_b64_tr_b16 a[248:249], v1 offset:14336\n\t"
    "ds_read_b64_tr_b16 a[250:251], v1 offset:15360\n\t"
    "ds_read_b64_tr_b16 a[252:253], v2 offset:14336\n\t"
    "ds_read_b64_tr_b16 a[254:255], v2 offset:15360\n\t"
    "v_rcp_f32 v28, v28\n\t"
    "v_mfma_f32_32x32x16_bf16 v[192:207], a[24:27], a[244:247], v[192:207]\n\t"
    "ds_read_b64_tr_b16 a[8:9], v3 offset:20480\n\t"
    "ds_read_b64_tr_b16 a[10:11], v3 offset:30720\n\t"
    "ds_read_b64_tr_b16 a[12:13], v3 offset:21504\n\t"
    "v_mul_f32 v27, v133, v27\n\t"
    "v_cvt_pk_bf16_f32 v130, v26, v27\n\t"
    "v_mul_f32 v32, s76, v138\n\t"
    "v_exp_f32 v31, v31\n\t"
    "v_mfma_f32_32x32x16_bf16 v[208:223], a[104:107], a[240:243], v[208:223]\n\t"
    "ds_read_b64_tr_b16 a[14:15], v3 offset:31744\n\t"
    "ds_read_b64_tr_b16 a[168:169], v5 offset:24576\n\t"
    "ds_read_b64_tr_b16 a[170:171], v5 offset:29696\n\t"
    "v_fma_f32 v30, v30, s77, s77\n\t"
    "v_rcp_f32 v29, v29\n\t"
    "v_mul_f32 v28, v134, v28\n\t"
    "v_mul_f32 v33, s76, v139\n\t"
    "v_mfma_f32_32x32x16_bf16 v[224:239], a[104:107], a[244:247], v[224:239]\n\t"
    "ds_read_b64_tr_b16 a[172:173], v5 offset:34816\n\t"
    "ds_read_b64_tr_b16 a[174:175], v5 offset:39936\n\t"
    "ds_read_b64_tr_b16 a[88:89], v3 offset:22528\n\t"
    "v_exp_f32 v32, v32\n\t"
    "v_fma_f32 v31, v31, s77, s77\n\t"
    "v_rcp_f32 v30, v30\n\t"
    "v_mul_f32 v29, v135, v29\n\t"
    "v_mfma_f32_32x32x16_bf16 v[240:255], a[184:187], a[240:243], v[240:255]\n\t"
    "ds_read_b64_tr_b16 a[90:91], v3 offset:32768\n\t"
    "ds_read_b64_tr_b16 a[92:93], v3 offset:23552\n\t"
    "ds_read_b64_tr_b16 a[94:95], v3 offset:33792\n\t"
    "s_add_u32 m0, s81, 81920\n\t"
    "s_add_u32 s60, s60, 20480\n\t"
    "s_addc_u32 s61, s61, 0\n\t"
    "v_cvt_pk_bf16_f32 v131, v28, v29\n\t"
    "s_waitcnt lgkmcnt(12)\n\t"
    "v_mfma_f32_32x32x16_bf16 v[176:191], a[28:31], a[248:251], v[176:191]\n\t"
    "global_load_lds_dwordx4 v7, s[60:61] offset:-2048\n\t"
    "ds_read_b64_tr_b16 a[240:241], v1 offset:16384\n\t"
    "ds_read_b64_tr_b16 a[242:243], v1 offset:17408\n\t"
    "ds_read_b64_tr_b16 a[244:245], v2 offset:16384\n\t"
    "ds_read_b64_tr_b16 a[246:247], v2 offset:17408\n\t"
    "v_mfma_f32_32x32x16_bf16 v[192:207], a[28:31], a[252:255], v[192:207]\n\t"
    "global_load_lds_dwordx4 v8, s[60:61] offset:-1024\n\t"
    "v_mul_f32 v34, s76, v140\n\t"
    "v_exp_f32 v33, v33\n\t"
    "v_fma_f32 v32, v32, s77, s77\n\t"
    "v_rcp_f32 v31, v31\n\t"
    "v_mul_f32 v30, v136, v30\n\t"
    "v_mfma_f32_32x32x16_bf16 v[208:223], a[108:111], a[248:251], v[208:223]\n\t"
    "global_load_lds_dwordx4 v9, s[60:61] offset:0\n\t"
    "ds_write_b128 v19, v[120:123] offset:2048\n\t"
    "v_mul_f32 v35, s76, v141\n\t"
    "v_exp_f32 v34, v34\n\t"
    "v_fma_f32 v33, v33, s77, s77\n\t"
    "v_rcp_f32 v32, v32\n\t"
    "v_mul_f32 v31, v137, v31\n\t"
    "v_mfma_f32_32x32x16_bf16 v[224:239], a[108:111], a[252:255], v[224:239]\n\t"
    "global_load_lds_dwordx4 v10, s[60:61] offset:1024\n\t"
    "v_cvt_pk_bf16_f32 v136, v30, v31\n\t"
    "v_mul_f32 v26, s76, v142\n\t"
    "v_exp_f32 v35, v35\n\t"
    "v_fma_f32 v34, v34, s77, s77\n\t"
    "v_rcp_f32 v33, v33\n\t"
    "v_mfma_f32_32x32x16_bf16 v[240:255], a[188:191], a[248:251], v[240:255]\n\t"
    "global_load_lds_dwordx4 v11, s[60:61] offset:2048\n\t"
    "v_mul_f32 v32, v138, v32\n\t"
    "v_mul_f32 v27, s76, v143\n\t"
    "v_exp_f32 v26, v26\n\t"
    "v_fma_f32 v35, v35, s77, s77\n\t"
    "v_rcp_f32 v34, v34\n\t"
    "v_mul_f32 v33, v139, v33\n\t"
    "s_waitcnt vmcnt(14) lgkmcnt(1)\n\t"
    "s_barrier\n\t"
    "v_mfma_f32_32x32x16_bf16 v[176:191], a[32:35], a[240:243], v[176:191]\n\t"
    "ds_read_b64_tr_b16 a[248:249], v1 offset:18432\n\t"
    "ds_read_b64_tr_b16 a[250:251], v1 offset:19456\n\t"
    "ds_read_b64_tr_b16 a[252:253], v2 offset:18432\n\t"
    "ds_read_b64_tr_b16 a[254:255], v2 offset:19456\n\t"
    "v_cvt_pk_bf16_f32 v137, v32, v33\n\t"
    "v_mfma_f32_32x32x16_bf16 v[192:207], a[32:35], a[244:247], v[192:207]\n\t"
    "ds_read_b64_tr_b16 a[16:17], v4 offset:0\n\t"
    "ds_read_b64_tr_b16 a[18:19], v4 offset:10240\n\t"
    "ds_read_b64_tr_b16 a[20:21], v4 offset:1024\n\t"
    "v_mul_f32 v28, s76, v144\n\t"
    "v_exp_f32 v27, v27\n\t"
    "v_fma_f32 v26, v26, s77, s77\n\t"
    "v_rcp_f32 v35, v35\n\t"
    "v_mfma_f32_32x32x16_bf16 v[208:223], a[112:115], a[240:243], v[208:223]\n\t"
    "ds_read_b64_tr_b16 a[22:23], v4 offset:11264\n\t"
    "ds_read_b64_tr_b16 a[176:177], v6 offset:4096\n\t"
    "ds_read_b64_tr_b16 a[178:179], v6 offset:9216\n\t"
    "v_mul_f32 v34, v140, v34\n\t"
    ".Ledm_hop1_%=:\n\t"
    "s_cbranch_execz .Ledm_ret%=\n\t"
    "v_mul_f32 v29, s76, v145\n\t"
    "v_exp_f32 v28, v28\n\t"
    "v_fma_f32 v27, v27, s77, s77\n\t"
    "v_mfma_f32_32x32x16_bf16 v[224:239], a[112:115], a[244:247], v[224:239]\n\t"
    "ds_read_b64_tr_b16 a[180:181], v6 offset:14336\n\t"
    "ds_read_b64_tr_b16 a[182:183], v6 offset:19456\n\t"
    "ds_read_b64_tr_b16 a[96:97], v4 offset:2048\n\t"
    "v_rcp_f32 v26, v26\n\t"
    "v_mul_f32 v35, v141, v35\n\t"
    "v_cvt_pk_bf16_f32 v138, v34, v35\n\t"
    "v_mul_f32 v30, s76, v146\n\t"
    "v_mfma_f32_32x32x16_bf16 v[240:255], a[192:195], a[240:243], v[240:255]\n\t"
    "ds_read_b64_tr_b16 a[98:99], v4 offset:12288\n\t"
    "ds_read_b64_tr_b16 a[100:101], v4 offset:3072\n\t"
    "ds_read_b64_tr_b16 a[102:103], v4 offset:13312\n\t"
    "s_add_u32 m0, s81, 102400\n\t"
    "s_add_u32 s60, s60, 20480\n\t"
    "s_addc_u32 s61, s61, 0\n\t"
    "v_exp_f32 v29, v29\n\t"
    "v_fma_f32 v28, v28, s77, s77\n\t"
    "s_waitcnt lgkmcnt(12)\n\t"
    "v_mfma_f32_32x32x16_bf16 v[176:191], a[36:39], a[248:251], v[176:191]\n\t"
    "global_load_lds_dwordx4 v7, s[60:61] offset:-2048\n\t"
    "ds_read_b64_tr_b16 a[240:241], v1 offset:20480\n\t"
    "ds_read_b64_tr_b16 a[242:243], v1 offset:21504\n\t"
    "ds_read_b64_tr_b16 a[244:245], v2 offset:20480\n\t"
    "ds_read_b64_tr_b16 a[246:247], v2 offset:21504\n\t"
    "v_rcp_f32 v27, v27\n\t"
    "v_mul_f32 v26, v142, v26\n\t"
    "v_mfma_f32_32x32x16_bf16 v[192:207], a[36:39], a[252:255], v[192:207]\n\t"
    "global_load_lds_dwordx4 v8, s[60:61] offset:-1024\n\t"
    "v_mul_f32 v31, s76, v147\n\t"
    "v_exp_f32 v30, v30\n\t"
    "v_fma_f32 v29, v29, s77, s77\n\t"
    "v_rcp_f32 v28, v28\n\t"
    "v_mul_f32 v27, v143, v27\n\t"
    "v_mfma_f32_32x32x16_bf16 v[208:223], a[116:119], a[248:251], v[208:223]\n\t"
    "global_load_lds_dwordx4 v9, s[60:61] offset:0\n\t"
    "ds_write_b128 v18, v[128:131] offset:16384\n\t"
    "v_cvt_pk_bf16_f32 v139, v26, v27\n\t"
    "v_mul_f32 v32, s76, v148\n\t"
    "v_exp_f32 v31, v31\n\t"
    "v_mfma_f32_32x32x16_bf16 v[224:239], a[116:119], a[252:255], v[224:239]\n\t"
    "global_load_lds_dwordx4 v10, s[60:61] offset:1024\n\t"
    "v_fma_f32 v30, v30, s77, s77\n\t"
    "v_rcp_f32 v29, v29\n\t"
    "v_mul_f32 v28, v144, v28\n\t"
    "v_mul_f32 v33, s76, v149\n\t"
    "v_exp_f32 v32, v32\n\t"
    "v_mfma_f32_32x32x16_bf16 v[240:255], a[196:199], a[248:251], v[240:255]\n\t"
    "global_load_lds_dwordx4 v11, s[60:61] offset:2048\n\t"
    "v_fma_f32 v31, v31, s77, s77\n\t"
    "v_rcp_f32 v30, v30\n\t"
    "v_mul_f32 v29, v145, v29\n\t"
    "v_cvt_pk_bf16_f32 v144, v28, v29\n\t"
    "v_mul_f32 v34, s76, v150\n\t"
    "s_waitcnt vmcnt(13) lgkmcnt(1)\n\t"
    "s_barrier\n\t"
    "v_mfma_f32_32x32x16_bf16 v[176:191], a[40:43], a[240:243], v[176:191]\n\t"
    "ds_read_b64_tr_b16 a[248:249], v1 offset:22528\n\t"
    "ds_read_b64_tr_b16 a[250:251], v1 offset:23552\n\t"
    "ds_read_b64_tr_b16 a[252:253], v2 offset:22528\n\t"
    "ds_read_b64_tr_b16 a[254:255], v2 offset:23552\n\t"
    "v_exp_f32 v33, v33\n\t"
    "v_mfma_f32_32x32x16_bf16 v[192:207], a[40:43], a[244:247], v[192:207]\n\t"
    "ds_read_b64_tr_b16 a[24:25], v4 offset:20480\n\t"
    "ds_read_b64_tr_b16 a[26:27], v4 offset:30720\n\t"
    "ds_read_b64_tr_b16 a[28:29], v4 offset:21504\n\t"
    "v_fma_f32 v32, v32, s77, s77\n\t"
    "v_rcp_f32 v31, v31\n\t"
    "v_mul_f32 v30, v146, v30\n\t"
    "v_mul_f32 v35, s76, v151\n\t"
    "v_mfma_f32_32x32x16_bf16 v[208:223], a[120:123], a[240:243], v[208:223]\n\t"
    "ds_read_b64_tr_b16 a[30:31], v4 offset:31744\n\t"
    "ds_read_b64_tr_b16 a[184:185], v6 offset:24576\n\t"
    "ds_read_b64_tr_b16 a[186:187], v6 offset:29696\n\t"
    "v_exp_f32 v34, v34\n\t"
    "v_fma_f32 v33, v33, s77, s77\n\t"
    "v_rcp_f32 v32, v32\n\t"
    "v_mfma_f32_32x32x16_bf16 v[224:239], a[120:123], a[244:247], v[224:239]\n\t"
    "ds_read_b64_tr_b16 a[188:189], v6 offset:34816\n\t"
    "ds_read_b64_tr_b16 a[190:191], v6 offset:39936\n\t"
    "ds_read_b64_tr_b16 a[104:105], v4 offset:22528\n\t"
    "v_mul_f32 v31, v147, v31\n\t"
    "v_cvt_pk_bf16_f32 v145, v30, v31\n\t"
    "v_mul_f32 v26, s76, v152\n\t"
    "v_exp_f32 v35, v35\n\t"
    "v_fma_f32 v34, v34, s77, s77\n\t"
    "v_mfma_f32_32x32x16_bf16 v[240:255], a[200:203], a[240:243], v[240:255]\n\t"
    "ds_read_b64_tr_b16 a[106:107], v4 offset:32768\n\t"
    "ds_read_b64_tr_b16 a[108:109], v4 offset:23552\n\t"
    "ds_read_b64_tr_b16 a[110:111], v4 offset:33792\n\t"
    "s_add_u32 m0, s81, 40960\n\t"
    "s_add_u32 s60, s60, 20480\n\t"
    "s_addc_u32 s61, s61, 0\n\t"
    "v_rcp_f32 v33, v33\n\t"
    "v_mul_f32 v32, v148, v32\n\t"
    "s_waitcnt lgkmcnt(12)\n\t"
    "v_mfma_f32_32x32x16_bf16 v[176:191], a[44:47], a[248:251], v[176:191]\n\t"
    "global_load_lds_dwordx4 v7, s[60:61] offset:-2048\n\t"
    "ds_read_b64_tr_b16 a[240:241], v1 offset:24576\n\t"
    "ds_read_b64_tr_b16 a[242:243], v1 offset:25600\n\t"
    "ds_read_b64_tr_b16 a[244:245], v2 offset:24576\n\t"
    "ds_read_b64_tr_b16 a[246:247], v2 offset:25600\n\t"
    "v_mul_f32 v27, s76, v153\n\t"
    "v_exp_f32 v26, v26\n\t"
    "v_mfma_f32_32x32x16_bf16 v[192:207], a[44:47], a[252:255], v[192:207]\n\t"
    "global_load_lds_dwordx4 v8, s[60:61] offset:-1024\n\t"
    "v_fma_f32 v35, v35, s77, s77\n\t"
    "v_rcp_f32 v34, v34\n\t"
    "v_mul_f32 v33, v149, v33\n\t"
    "v_cvt_pk_bf16_f32 v146, v32, v33\n\t"
    "v_mul_f32 v28, s76, v154\n\t"
    "v_mfma_f32_32x32x16_bf16 v[208:223], a[124:127], a[248:251], v[208:223]\n\t"
    "global_load_lds_dwordx4 v9, s[60:61] offset:0\n\t"
    "ds_write_b128 v18, v[136:139] offset:18432\n\t"
    "v_exp_f32 v27, v27\n\t"
    "v_fma_f32 v26, v26, s77, s77\n\t"
    "v_rcp_f32 v35, v35\n\t"
    "v_mul_f32 v34, v150, v34\n\t"
    "v_mul_f32 v29, s76, v155\n\t"
    "v_mfma_f32_32x32x16_bf16 v[224:239], a[124:127], a[252:255], v[224:239]\n\t"
    "global_load_lds_dwordx4 v10, s[60:61] offset:1024\n\t"
    "v_exp_f32 v28, v28\n\t"
    "v_fma_f32 v27, v27, s77, s77\n\t"
    "v_rcp_f32 v26, v26\n\t"
    "v_mul_f32 v35, v151, v35\n\t"
    "v_cvt_pk_bf16_f32 v147, v34, v35\n\t"
    "v_mfma_f32_32x32x16_bf16 v[240:255], a[204:207], a[248:251], v[240:255]\n\t"
    "global_load_lds_dwordx4 v11, s[60:61] offset:2048\n\t"
    "v_mul_f32 v30, s76, v156\n\t"
    "v_exp_f32 v29, v29\n\t"
    "v_fma_f32 v28, v28, s77, s77\n\t"
    "s_waitcnt vmcnt(10) lgkmcnt(1)\n\t"
    "s_barrier\n\t"
    "v_mfma_f32_32x32x16_bf16 v[176:191], a[48:51], a[240:243], v[176:191]\n\t"
    "ds_read_b64_tr_b16 a[248:249], v1 offset:26624\n\t"
    "ds_read_b64_tr_b16 a[250:251], v1 offset:27648\n\t"
    "ds_read_b64_tr_b16 a[252:253], v2 offset:26624\n\t"
    "ds_read_b64_tr_b16 a[254:255], v2 offset:27648\n\t"
    "v_rcp_f32 v27, v27\n\t"
    "v_mfma_f32_32x32x16_bf16 v[192:207], a[48:51], a[244:247], v[192:207]\n\t"
    "ds_read_b64_tr_b16 a[32:33], v3 offset:0\n\t"
    "ds_read_b64_tr_b16 a[34:35], v3 offset:10240\n\t"
    "ds_read_b64_tr_b16 a[36:37], v3 offset:1024\n\t"
    "v_mul_f32 v26, v152, v26\n\t"
    "v_mul_f32 v31, s76, v157\n\t"
    "v_exp_f32 v30, v30\n\t"
    "v_fma_f32 v29, v29, s77, s77\n\t"
    "v_mfma_f32_32x32x16_bf16 v[208:223], a[128:131], a[240:243], v[208:223]\n\t"
    "ds_read_b64_tr_b16 a[38:39], v3 offset:11264\n\t"
    "ds_read_b64_tr_b16 a[192:193], v5 offset:4096\n\t"
    "ds_read_b64_tr_b16 a[194:195], v5 offset:9216\n\t"
    "v_rcp_f32 v28, v28\n\t"
    "v_mul_f32 v27, v153, v27\n\t"
    "v_cvt_pk_bf16_f32 v152, v26, v27\n\t"
    "v_mul_f32 v32, s76, v158\n\t"
    "v_mfma_f32_32x32x16_bf16 v[224:239], a[128:131], a[244:247], v[224:239]\n\t"
    "ds_read_b64_tr_b16 a[196:197], v5 offset:14336\n\t"
    "ds_read_b64_tr_b16 a[198:199], v5 offset:19456\n\t"
    "ds_read_b64_tr_b16 a[112:113], v3 offset:2048\n\t"
    "v_exp_f32 v31, v31\n\t"
    "v_fma_f32 v30, v30, s77, s77\n\t"
    "v_rcp_f32 v29, v29\n\t"
    "v_mul_f32 v28, v154, v28\n\t"
    "v_mfma_f32_32x32x16_bf16 v[240:255], a[208:211], a[240:243], v[240:255]\n\t"
    "ds_read_b64_tr_b16 a[114:115], v3 offset:12288\n\t"
    "ds_read_b64_tr_b16 a[116:117], v3 offset:3072\n\t"
    "ds_read_b64_tr_b16 a[118:119], v3 offset:13312\n\t"
    "s_add_u32 m0, s81, 61440\n\t"
    "s_add_u32 s60, s60, 20480\n\t"
    "s_addc_u32 s61, s61, 0\n\t"
    "v_mul_f32 v33, s76, v159\n\t"
    "v_exp_f32 v32, v32\n\t"
    "s_waitcnt lgkmcnt(12)\n\t"
    "v_mfma_f32_32x32x16_bf16 v[176:191], a[52:55], a[248:251], v[176:191]\n\t"
    "global_load_lds_dwordx4 v7, s[60:61] offset:-2048\n\t"
    "ds_read_b64_tr_b16 a[240:241], v1 offset:28672\n\t"
    "ds_read_b64_tr_b16 a[242:243], v1 offset:29696\n\t"
    "ds_read_b64_tr_b16 a[244:245], v2 offset:28672\n\t"
    "ds_read_b64_tr_b16 a[246:247], v2 offset:29696\n\t"
    "v_fma_f32 v31, v31, s77, s77\n\t"
    "v_rcp_f32 v30, v30\n\t"
    "v_mfma_f32_32x32x16_bf16 v[192:207], a[52:55], a[252:255], v[192:207]\n\t"
    "global_load_lds_dwordx4 v8, s[60:61] offset:-1024\n\t"
    "v_mul_f32 v29, v155, v29\n\t"
    "v_cvt_pk_bf16_f32 v153, v28, v29\n\t"
    "v_mul_f32 v34, s76, v160\n\t"
    "v_exp_f32 v33, v33\n\t"
    "v_fma_f32 v32, v32, s77, s77\n\t"
    "v_mfma_f32_32x32x16_bf16 v[208:223], a[132:135], a[248:251], v[208:223]\n\t"
    "global_load_lds_dwordx4 v9, s[60:61] offset:0\n\t"
    "ds_write_b128 v19, v[144:147] offset:16384\n\t"
    "v_rcp_f32 v31, v31\n\t"
    "v_mul_f32 v30, v156, v30\n\t"
    "v_mul_f32 v35, s76, v161\n\t"
    "v_exp_f32 v34, v34\n\t"
    "v_fma_f32 v33, v33, s77, s77\n\t"
    "v_mfma_f32_32x32x16_bf16 v[224:239], a[132:135], a[252:255], v[224:239]\n\t"
    "global_load_lds_dwordx4 v10, s[60:61] offset:1024\n\t"
    "v_rcp_f32 v32, v32\n\t"
    "v_mul_f32 v31, v157, v31\n\t"
    "v_cvt_pk_bf16_f32 v154, v30, v31\n\t"
    "v_mul_f32 v26, s76, v162\n\t"
    "v_exp_f32 v35, v35\n\t"
    "v_fma_f32 v34, v34, s77, s77\n\t"
    "v_mfma_f32_32x32x16_bf16 v[240:255], a[212:215], a[248:251], v[240:255]\n\t"
    "global_load_lds_dwordx4 v11, s[60:61] offset:2048\n\t"
    "v_rcp_f32 v33, v33\n\t"
    "v_mul_f32 v32, v158, v32\n\t"
    "v_mul_f32 v27, s76, v163\n\t"
    "v_exp_f32 v26, v26\n\t"
    "v_fma_f32 v35, v35, s77, s77\n\t"
    "s_waitcnt vmcnt(10) lgkmcnt(1)\n\t"
    "s_barrier\n\t"
    "v_mfma_f32_32x32x16_bf16 v[176:191], a[56:59], a[240:243], v[176:191]\n\t"
    "ds_read_b64_tr_b16 a[248:249], v1 offset:30720\n\t"
    "ds_read_b64_tr_b16 a[250:251], v1 offset:31744\n\t"
    "ds_read_b64_tr_b16 a[252:253], v2 offset:30720\n\t"
    "ds_read_b64_tr_b16 a[254:255], v2 offset:31744\n\t"
    "v_rcp_f32 v34, v34\n\t"
    "v_mfma_f32_32x32x16_bf16 v[192:207], a[56:59], a[244:247], v[192:207]\n\t"
    "ds_read_b64_tr_b16 a[40:41], v3 offset:20480\n\t"
    "ds_read_b64_tr_b16 a[42:43], v3 offset:30720\n\t"
    "ds_read_b64_tr_b16 a[44:45], v3 offset:21504\n\t"
    "v_mul_f32 v33, v159, v33\n\t"
    "v_cvt_pk_bf16_f32 v155, v32, v33\n\t"
    "v_mfma_f32_32x32x16_bf16 v[208:223], a[136:139], a[240:243], v[208:223]\n\t"
    "ds_read_b64_tr_b16 a[46:47], v3 offset:31744\n\t"
    "ds_read_b64_tr_b16 a[200:201], v5 offset:24576\n\t"
    "ds_read_b64_tr_b16 a[202:203], v5 offset:29696\n\t"
    "v_mul_f32 v28, s76, v164\n\t"
    "v_exp_f32 v27, v27\n\t"
    "v_fma_f32 v26, v26, s77, s77\n\t"
    "v_mfma_f32_32x32x16_bf16 v[224:239], a[136:139], a[244:247], v[224:239]\n\t"
    "ds_read_b64_tr_b16 a[204:205], v5 offset:34816\n\t"
    "ds_read_b64_tr_b16 a[206:207], v5 offset:39936\n\t"
    "ds_read_b64_tr_b16 a[120:121], v3 offset:22528\n\t"
    "v_rcp_f32 v35, v35\n\t"
    "v_mul_f32 v34, v160, v34\n\t"
    "v_mul_f32 v29, s76, v165\n\t"
    "v_exp_f32 v28, v28\n\t"
    "v_mfma_f32_32x32x16_bf16 v[240:255], a[216:219], a[240:243], v[240:255]\n\t"
    "ds_read_b64_tr_b16 a[122:123], v3 offset:32768\n\t"
    "ds_read_b64_tr_b16 a[124:125], v3 offset:23552\n\t"
    "ds_read_b64_tr_b16 a[126:127], v3 offset:33792\n\t"
    "s_add_u32 m0, s81, 81920\n\t"
    "s_add_u32 s60, s60, 20480\n\t"
    "s_addc_u32 s61, s61, 0\n\t"
    "v_fma_f32 v27, v27, s77, s77\n\t"
    "v_rcp_f32 v26, v26\n\t"
    "s_waitcnt lgkmcnt(12)\n\t"
    "v_mfma_f32_32x32x16_bf16 v[176:191], a[60:63], a[248:251], v[176:191]\n\t"
    "global_load_lds_dwordx4 v7, s[60:61] offset:-2048\n\t"
    "ds_read_b64_tr_b16 a[240:241], v1 offset:32768\n\t"
    "ds_read_b64_tr_b16 a[242:243], v1 offset:33792\n\t"
    "ds_read_b64_tr_b16 a[244:245], v2 offset:32768\n\t"
    "ds_read_b64_tr_b16 a[246:247], v2 offset:33792\n\t"
    "v_mul_f32 v35, v161, v35\n\t"
    "v_cvt_pk_bf16_f32 v160, v34, v35\n\t"
    "v_mfma_f32_32x32x16_bf16 v[192:207], a[60:63], a[252:255], v[192:207]\n\t"
    "global_load_lds_dwordx4 v8, s[60:61] offset:-1024\n\t"
    "v_mul_f32 v30, s76, v166\n\t"
    "v_exp_f32 v29, v29\n\t"
    "v_fma_f32 v28, v28, s77, s77\n\t"
    "v_rcp_f32 v27, v27\n\t"
    "v_mul_f32 v26, v162, v26\n\t"
    "v_mfma_f32_32x32x16_bf16 v[208:223], a[140:143], a[248:251], v[208:223]\n\t"
    "global_load_lds_dwordx4 v9, s[60:61] offset:0\n\t"
    "ds_write_b128 v19, v[152:155] offset:18432\n\t"
    "v_mul_f32 v31, s76, v167\n\t"
    "v_exp_f32 v30, v30\n\t"
    "v_fma_f32 v29, v29, s77, s77\n\t"
    "v_rcp_f32 v28, v28\n\t"
    "v_mul_f32 v27, v163, v27\n\t"
    "v_cvt_pk_bf16_f32 v161, v26, v27\n\t"
    "v_mfma_f32_32x32x16_bf16 v[224:239], a[140:143], a[252:255], v[224:239]\n\t"
    "global_load_lds_dwordx4 v10, s[60:61] offset:1024\n\t"
    "v_mul_f32 v32, s76, v168\n\t"
    "v_exp_f32 v31, v31\n\t"
    "v_fma_f32 v30, v30, s77, s77\n\t"
    "v_rcp_f32 v29, v29\n\t"
    "v_mul_f32 v28, v164, v28\n\t"
    "v_mfma_f32_32x32x16_bf16 v[240:255], a[220:223], a[248:251], v[240:255]\n\t"
    "global_load_lds_dwordx4 v11, s[60:61] offset:2048\n\t"
    "v_mul_f32 v33, s76, v169\n\t"
    "v_exp_f32 v32, v32\n\t"
    "v_fma_f32 v31, v31, s77, s77\n\t"
    "v_rcp_f32 v30, v30\n\t"
    "v_mul_f32 v29, v165, v29\n\t"
    "s_waitcnt vmcnt(10) lgkmcnt(1)\n\t"
    "s_barrier\n\t"
    "v_mfma_f32_32x32x16_bf16 v[176:191], a[64:67], a[240:243], v[176:191]\n\t"
    "ds_read_b64_tr_b16 a[248:249], v1 offset:34816\n\t"
    "ds_read_b64_tr_b16 a[250:251], v1 offset:35840\n\t"
    "ds_read_b64_tr_b16 a[252:253], v2 offset:34816\n\t"
    "ds_read_b64_tr_b16 a[254:255], v2 offset:35840\n\t"
    "v_cvt_pk_bf16_f32 v162, v28, v29\n\t"
    "v_mul_f32 v34, s76, v170\n\t"
    "v_mfma_f32_32x32x16_bf16 v[192:207], a[64:67], a[244:247], v[192:207]\n\t"
    "ds_read_b64_tr_b16 a[48:49], v4 offset:0\n\t"
    "ds_read_b64_tr_b16 a[50:51], v4 offset:10240\n\t"
    "ds_read_b64_tr_b16 a[52:53], v4 offset:1024\n\t"
    "v_exp_f32 v33, v33\n\t"
    "v_fma_f32 v32, v32, s77, s77\n\t"
    "v_rcp_f32 v31, v31\n\t"
    "v_mfma_f32_32x32x16_bf16 v[208:223], a[144:147], a[240:243], v[208:223]\n\t"
    "ds_read_b64_tr_b16 a[54:55], v4 offset:11264\n\t"
    "ds_read_b64_tr_b16 a[208:209], v6 offset:4096\n\t"
    "ds_read_b64_tr_b16 a[210:211], v6 offset:9216\n\t"
    "v_mul_f32 v30, v166, v30\n\t"
    "v_mul_f32 v35, s76, v171\n\t"
    "v_exp_f32 v34, v34\n\t"
    "v_fma_f32 v33, v33, s77, s77\n\t"
    "v_mfma_f32_32x32x16_bf16 v[224:239], a[144:147], a[244:247], v[224:239]\n\t"
    "ds_read_b64_tr_b16 a[212:213], v6 offset:14336\n\t"
    "ds_read_b64_tr_b16 a[214:215], v6 offset:19456\n\t"
    "ds_read_b64_tr_b16 a[128:129], v4 offset:2048\n\t"
    "v_rcp_f32 v32, v32\n\t"
    "v_mul_f32 v31, v167, v31\n\t"
    "v_cvt_pk_bf16_f32 v163, v30, v31\n\t"
    "v_mfma_f32_32x32x16_bf16 v[240:255], a[224:227], a[240:243], v[240:255]\n\t"
    "ds_read_b64_tr_b16 a[130:131], v4 offset:12288\n\t"
    "ds_read_b64_tr_b16 a[132:133], v4 offset:3072\n\t"
    "ds_read_b64_tr_b16 a[134:135], v4 offset:13312\n\t"
    "s_add_u32 m0, s81, 102400\n\t"
    "s_add_u32 s60, s60, 20480\n\t"
    "s_addc_u32 s61, s61, 0\n\t"
    "v_mul_f32 v26, s76, v172\n\t"
    "s_waitcnt lgkmcnt(12)\n\t"
    "v_mfma_f32_32x32x16_bf16 v[176:191], a[68:71], a[248:251], v[176:191]\n\t"
    "global_load_lds_dwordx4 v7, s[60:61] offset:-2048\n\t"
    "ds_read_b64_tr_b16 a[240:241], v1 offset:36864\n\t"
    "ds_read_b64_tr_b16 a[242:243], v1 offset:37888\n\t"
    "ds_read_b64_tr_b16 a[244:245], v2 offset:36864\n\t"
    "ds_read_b64_tr_b16 a[246:247], v2 offset:37888\n\t"
    "v_exp_f32 v35, v35\n\t"
    "v_mfma_f32_32x32x16_bf16 v[192:207], a[68:71], a[252:255], v[192:207]\n\t"
    "global_load_lds_dwordx4 v8, s[60:61] offset:-1024\n\t"
    "v_fma_f32 v34, v34, s77, s77\n\t"
    "v_rcp_f32 v33, v33\n\t"
    "v_mul_f32 v32, v168, v32\n\t"
    "v_mul_f32 v27, s76, v173\n\t"
    "v_exp_f32 v26, v26\n\t"
    "v_mfma_f32_32x32x16_bf16 v[208:223], a[148:151], a[248:251], v[208:223]\n\t"
    "global_load_lds_dwordx4 v9, s[60:61] offset:0\n\t"
    "ds_write_b128 v20, v[160:163] offset:0\n\t"
    "v_fma_f32 v35, v35, s77, s77\n\t"
    "v_rcp_f32 v34, v34\n\t"
    "v_mul_f32 v33, v169, v33\n\t"
    "v_cvt_pk_bf16_f32 v168, v32, v33\n\t"
    "v_mul_f32 v28, s76, v174\n\t"
    "v_exp_f32 v27, v27\n\t"
    "v_mfma_f32_32x32x16_bf16 v[224:239], a[148:151], a[252:255], v[224:239]\n\t"
    "global_load_lds_dwordx4 v10, s[60:61] offset:1024\n\t"
    "v_fma_f32 v26, v26, s77, s77\n\t"
    "v_rcp_f32 v35, v35\n\t"
    "v_mul_f32 v34, v170, v34\n\t"
    "v_mul_f32 v29, s76, v175\n\t"
    "v_exp_f32 v28, v28\n\t"
    "v_mfma_f32_32x32x16_bf16 v[240:255], a[228:231], a[248:251], v[240:255]\n\t"
    "global_load_lds_dwordx4 v11, s[60:61] offset:2048\n\t"
    "v_fma_f32 v27, v27, s77, s77\n\t"
    "v_rcp_f32 v26, v26\n\t"
    "v_mul_f32 v35, v171, v35\n\t"
    "v_cvt_pk_bf16_f32 v169, v34, v35\n\t"
    "v_exp_f32 v29, v29\n\t"
    "s_waitcnt vmcnt(10) lgkmcnt(1)\n\t"
    "s_barrier\n\t"
    "v_mfma_f32_32x32x16_bf16 v[176:191], a[72:75], a[240:243], v[176:191]\n\t"
    "ds_read_b64_tr_b16 a[248:249], v1 offset:38912\n\t"
    "ds_read_b64_tr_b16 a[250:251], v1 offset:39936\n\t"
    "ds_read_b64_tr_b16 a[252:253], v2 offset:38912\n\t"
    "ds_read_b64_tr_b16 a[254:255], v2 offset:39936\n\t"
    "v_fma_f32 v28, v28, s77, s77\n\t"
    "v_mfma_f32_32x32x16_bf16 v[192:207], a[72:75], a[244:247], v[192:207]\n\t"
    "ds_read_b64_tr_b16 a[56:57], v4 offset:20480\n\t"
    "ds_read_b64_tr_b16 a[58:59], v4 offset:30720\n\t"
    "ds_read_b64_tr_b16 a[60:61], v4 offset:21504\n\t"
    "v_rcp_f32 v27, v27\n\t"
    "v_mul_f32 v26, v172, v26\n\t"
    "v_fma_f32 v29, v29, s77, s77\n\t"
    "v_rcp_f32 v28, v28\n\t"
    "v_mfma_f32_32x32x16_bf16 v[208:223], a[152:155], a[240:243], v[208:223]\n\t"
    "ds_read_b64_tr_b16 a[62:63], v4 offset:31744\n\t"
    "ds_read_b64_tr_b16 a[216:217], v6 offset:24576\n\t"
    "ds_read_b64_tr_b16 a[218:219], v6 offset:29696\n\t"
    "v_mul_f32 v27, v173, v27\n\t"
    "v_cvt_pk_bf16_f32 v170, v26, v27\n\t"
    "v_rcp_f32 v29, v29\n\t"
    "v_mul_f32 v28, v174, v28\n\t"
    "v_mul_f32 v29, v175, v29\n\t"
    "v_mfma_f32_32x32x16_bf16 v[224:239], a[152:155], a[244:247], v[224:239]\n\t"
    "ds_read_b64_tr_b16 a[220:221], v6 offset:34816\n\t"
    "ds_read_b64_tr_b16 a[222:223], v6 offset:39936\n\t"
    "ds_read_b64_tr_b16 a[136:137], v4 offset:22528\n\t"
    "v_cvt_pk_bf16_f32 v171, v28, v29\n\t"
    "v_mfma_f32_32x32x16_bf16 v[240:255], a[232:235], a[240:243], v[240:255]\n\t"
    "ds_read_b64_tr_b16 a[138:139], v4 offset:32768\n\t"
    "ds_read_b64_tr_b16 a[140:141], v4 offset:23552\n\t"
    "ds_read_b64_tr_b16 a[142:143], v4 offset:33792\n\t"
    "s_waitcnt lgkmcnt(12)\n\t"
    "v_mfma_f32_32x32x16_bf16 v[176:191], a[76:79], a[248:251], v[176:191]\n\t"
    "v_mfma_f32_32x32x16_bf16 v[192:207], a[76:79], a[252:255], v[192:207]\n\t"
    "v_mfma_f32_32x32x16_bf16 v[208:223], a[156:159], a[248:251], v[208:223]\n\t"
    "ds_write_b128 v20, v[168:171] offset:2048\n\t"
    "v_mfma_f32_32x32x16_bf16 v[224:239], a[156:159], a[252:255], v[224:239]\n\t"
    "v_add_u32 v44, s86, v1\n\t"
    "v_add_u32 v45, s86, v2\n\t"
    "v_add_u32 v46, s87, v1\n\t"
    "v_mfma_f32_32x32x16_bf16 v[240:255], a[236:239], a[248:251], v[240:255]\n\t"
    "v_subrev_u32 v18, 122880, v18\n\t"
    "v_subrev_u32 v19, 122880, v19\n\t"
    "v_subrev_u32 v20, 122880, v20\n\t"
    "s_waitcnt vmcnt(5) lgkmcnt(1)\n\t"
    "s_barrier\n\t"
    "v_mfma_f32_32x32x16_bf16 v[96:111], v[56:59], v[36:39], 0\n\t"
    "ds_read_b64_tr_b16 v[56:57], v44 offset:0\n\t"
    "ds_read_b64_tr_b16 v[58:59], v44 offset:1024\n\t"
    "ds_read_b64_tr_b16 a[64:65], v3 offset:0\n\t"
    "ds_read_b64_tr_b16 a[66:67], v3 offset:10240\n\t"
    "ds_read_b64_tr_b16 a[68:69], v3 offset:1024\n\t"
    "v_mfma_f32_32x32x16_bf16 v[112:127], v[64:67], v[36:39], 0\n\t"
    "ds_read_b64_tr_b16 v[64:65], v45 offset:0\n\t"
    "ds_read_b64_tr_b16 v[66:67], v45 offset:1024\n\t"
    "ds_read_b64_tr_b16 a[70:71], v3 offset:11264\n\t"
    "ds_read_b64_tr_b16 a[224:225], v5 offset:4096\n\t"
    "ds_read_b64_tr_b16 a[226:227], v5 offset:9216\n\t"
    "v_mul_f32 v26, s76, v176\n\t"
    "v_mul_f32 v27, s76, v177\n\t"
    "v_mfma_f32_32x32x16_bf16 v[128:143], v[72:75], v[36:39], 0\n\t"
    "ds_read_b64_tr_b16 v[72:73], v44 offset:16384\n\t"
    "ds_read_b64_tr_b16 v[74:75], v44 offset:17408\n\t"
    "ds_read_b64_tr_b16 a[228:229], v5 offset:14336\n\t"
    "ds_read_b64_tr_b16 a[230:231], v5 offset:19456\n\t"
    "ds_read_b64_tr_b16 a[144:145], v3 offset:2048\n\t"
    "v_exp_f32 v26, v26\n\t"
    "v_mfma_f32_32x32x16_bf16 v[144:159], v[80:83], v[36:39], 0\n\t"
    "ds_read_b64_tr_b16 v[80:81], v45 offset:16384\n\t"
    "ds_read_b64_tr_b16 v[82:83], v45 offset:17408\n\t"
    "ds_read_b64_tr_b16 a[146:147], v3 offset:12288\n\t"
    "ds_read_b64_tr_b16 a[148:149], v3 offset:3072\n\t"
    "ds_read_b64_tr_b16 a[150:151], v3 offset:13312\n\t"
    "v_mul_f32 v28, s76, v178\n\t"
    "v_mfma_f32_32x32x16_bf16 v[96:111], v[60:63], v[40:43], v[96:111]\n\t"
    "ds_read_b64_tr_b16 v[60:61], v44 offset:2048\n\t"
    "ds_read_b64_tr_b16 v[62:63], v44 offset:3072\n\t"
    "ds_read_b128 a[240:243], v16 offset:0\n\t"
    "v_exp_f32 v27, v27\n\t"
    "v_fma_f32 v26, v26, s77, s77\n\t"
    "v_mfma_f32_32x32x16_bf16 v[112:127], v[68:71], v[40:43], v[112:127]\n\t"
    "ds_read_b64_tr_b16 v[68:69], v45 offset:2048\n\t"
    "ds_read_b64_tr_b16 v[70:71], v45 offset:3072\n\t"
    "ds_read_b128 a[244:247], v16 offset:2048\n\t"
    "v_mul_f32 v29, s76, v179\n\t"
    "v_exp_f32 v28, v28\n\t"
    "v_fma_f32 v27, v27, s77, s77\n\t"
    "v_mfma_f32_32x32x16_bf16 v[128:143], v[76:79], v[40:43], v[128:143]\n\t"
    "ds_read_b64_tr_b16 v[76:77], v44 offset:18432\n\t"
    "ds_read_b64_tr_b16 v[78:79], v44 offset:19456\n\t"
    "ds_read_b128 a[248:251], v16 offset:4096\n\t"
    "v_rcp_f32 v26, v26\n\t"
    "v_mul_f32 v30, s76, v180\n\t"
    "v_mfma_f32_32x32x16_bf16 v[144:159], v[84:87], v[40:43], v[144:159]\n\t"
    "ds_read_b64_tr_b16 v[84:85], v45 offset:18432\n\t"
    "ds_read_b64_tr_b16 v[86:87], v45 offset:19456\n\t"
    "ds_read_b128 a[252:255], v16 offset:6144\n\t"
    "v_exp_f32 v29, v29\n\t"
    "v_fma_f32 v28, v28, s77, s77\n\t"
    "v_rcp_f32 v27, v27\n\t"
    "s_waitcnt lgkmcnt(15)\n\t"
    "v_mfma_f32_32x32x16_bf16 v[160:175], v[88:91], v[36:39], 0\n\t"
    "ds_read_b64_tr_b16 v[88:89], v46 offset:0\n\t"
    "ds_read_b64_tr_b16 v[90:91], v46 offset:1024\n\t"
    "ds_read_b128 v[48:51], v16 offset:8192\n\t"
    "v_mul_f32 v26, v176, v26\n\t"
    "v_mul_f32 v31, s76, v181\n\t"
    "v_mfma_f32_32x32x16_bf16 v[160:175], v[92:95], v[40:43], v[160:175]\n\t"
    "ds_read_b64_tr_b16 v[92:93], v46 offset:2048\n\t"
    "ds_read_b64_tr_b16 v[94:95], v46 offset:3072\n\t"
    "ds_read_b128 v[52:55], v16 offset:10240\n\t"
    "v_exp_f32 v30, v30\n\t"
    "v_fma_f32 v29, v29, s77, s77\n\t"
    "s_waitcnt vmcnt(0) lgkmcnt(1)\n\t"
    "s_barrier\n\t"
    "v_mfma_f32_32x32x16_bf16 v[96:111], a[240:243], a[0:3], v[96:111]\n\t"
    "ds_read_b128 a[240:243], v16 offset:12288\n\t"
    "ds_read_b64_tr_b16 a[72:73], v3 offset:20480\n\t"
    "ds_read_b64_tr_b16 a[74:75], v3 offset:30720\n\t"
    "ds_read_b64_tr_b16 a[76:77], v3 offset:21504\n\t"
    "v_mfma_f32_32x32x16_bf16 v[96:111], a[244:247], a[4:7], v[96:111]\n\t"
    "ds_read_b128 a[244:247], v16 offset:14336\n\t"
    "ds_read_b64_tr_b16 a[78:79], v3 offset:31744\n\t"
    "ds_read_b64_tr_b16 a[232:233], v5 offset:24576\n\t"
    "ds_read_b64_tr_b16 a[234:235], v5 offset:29696\n\t"
    "v_rcp_f32 v28, v28\n\t"
    "v_mul_f32 v27, v177, v27\n\t"
    "v_mfma_f32_32x32x16_bf16 v[96:111], a[248:251], a[8:11], v[96:111]\n\t"
    "ds_read_b128 a[248:251], v16 offset:16384\n\t"
    "ds_read_b64_tr_b16 a[236:237], v5 offset:34816\n\t"
    "ds_read_b64_tr_b16 a[238:239], v5 offset:39936\n\t"
    "ds_read_b64_tr_b16 a[152:153], v3 offset:22528\n\t"
    "v_cvt_pk_bf16_f32 v176, v26, v27\n\t"
    "v_mfma_f32_32x32x16_bf16 v[96:111], a[252:255], a[12:15], v[96:111]\n\t"
    "ds_read_b128 a[252:255], v16 offset:18432\n\t"
    "ds_read_b64_tr_b16 a[154:155], v3 offset:32768\n\t"
    "ds_read_b64_tr_b16 a[156:157], v3 offset:23552\n\t"
    "ds_read_b64_tr_b16 a[158:159], v3 offset:33792\n\t"
    "v_mul_f32 v32, s76, v182\n\t"
    "s_waitcnt lgkmcnt(15)\n\t"
    "v_mfma_f32_32x32x16_bf16 v[96:111], v[48:51], a[16:19], v[96:111]\n\t"
    "ds_read_b128 v[48:51], v16 offset:20480\n\t"
    "v_exp_f32 v31, v31\n\t"
    "v_fma_f32 v30, v30, s77, s77\n\t"
    "v_rcp_f32 v29, v29\n\t"
    "v_mfma_f32_32x32x16_bf16 v[96:111], v[52:55], a[20:23], v[96:111]\n\t"
    "ds_read_b128 v[52:55], v16 offset:22528\n\t"
    "v_mul_f32 v28, v178, v28\n\t"
    "v_mul_f32 v33, s76, v183\n\t"
    "v_exp_f32 v32, v32\n\t"
    "v_fma_f32 v31, v31, s77, s77\n\t"
    "s_waitcnt lgkmcnt(13)\n\t"
    "v_mfma_f32_32x32x16_bf16 v[96:111], a[240:243], a[24:27], v[96:111]\n\t"
    "ds_read_b128 a[240:243], v16 offset:24576\n\t"
    "v_rcp_f32 v30, v30\n\t"
    "v_mul_f32 v29, v179, v29\n\t"
    "v_cvt_pk_bf16_f32 v177, v28, v29\n\t"
    "v_mfma_f32_32x32x16_bf16 v[96:111], a[244:247], a[28:31], v[96:111]\n\t"
    "ds_read_b128 a[244:247], v16 offset:26624\n\t"
    "v_mul_f32 v34, s76, v184\n\t"
    "v_exp_f32 v33, v33\n\t"
    "v_fma_f32 v32, v32, s77, s77\n\t"
    "s_waitcnt lgkmcnt(7)\n\t"
    "v_mfma_f32_32x32x16_bf16 v[96:111], a[248:251], a[32:35], v[96:111]\n\t"
    "ds_read_b128 a[248:251], v16 offset:28672\n\t"
    "v_rcp_f32 v31, v31\n\t"
    "v_mul_f32 v30, v180, v30\n\t"
    "v_mul_f32 v35, s76, v185\n\t"
    "v_mfma_f32_32x32x16_bf16 v[96:111], a[252:255], a[36:39], v[96:111]\n\t"
    "ds_read_b128 a[252:255], v16 offset:30720\n\t"
    "v_exp_f32 v34, v34\n\t"
    "v_fma_f32 v33, v33, s77, s77\n\t"
    "v_rcp_f32 v32, v32\n\t"
    "v_mul_f32 v31, v181, v31\n\t"
    "s_waitcnt lgkmcnt(4)\n\t"
    "v_mfma_f32_32x32x16_bf16 v[96:111], v[48:51], a[40:43], v[96:111]\n\t"
    "ds_read_b128 v[48:51], v16 offset:32768\n\t"
    "v_cvt_pk_bf16_f32 v178, v30, v31\n\t"
    "v_mul_f32 v26, s76, v186\n\t"
    "v_exp_f32 v35, v35\n\t"
    "v_mfma_f32_32x32x16_bf16 v[96:111], v[52:55], a[44:47], v[96:111]\n\t"
    "ds_read_b128 v[52:55], v16 offset:34816\n\t"
    "v_fma_f32 v34, v34, s77, s77\n\t"
    "v_rcp_f32 v33, v33\n\t"
    "v_mul_f32 v32, v182, v32\n\t"
    "s_waitcnt lgkmcnt(4)\n\t"
    "v_mfma_f32_32x32x16_bf16 v[96:111], a[240:243], a[48:51], v[96:111]\n\t"
    "ds_read_b128 a[240:243], v16 offset:36864\n\t"
    "v_mul_f32 v27, s76, v187\n\t"
    "v_exp_f32 v26, v26\n\t"
    "v_fma_f32 v35, v35, s77, s77\n\t"
    "v_mfma_f32_32x32x16_bf16 v[96:111], a[244:247], a[52:55], v[96:111]\n\t"
    "ds_read_b128 a[244:247], v16 offset:38912\n\t"
    "v_rcp_f32 v34, v34\n\t"
    "v_mul_f32 v33, v183, v33\n\t"
    "v_cvt_pk_bf16_f32 v179, v32, v33\n\t"
    "s_waitcnt lgkmcnt(4)\n\t"
    "v_mfma_f32_32x32x16_bf16 v[96:111], a[248:251], a[56:59], v[96:111]\n\t"
    "ds_read_b128 a[248:251], v17 offset:0\n\t"
    "ds_write_b128 v18, v[176:179] offset:0\n\t"
    "v_mul_f32 v28, s76, v188\n\t"
    "v_mfma_f32_32x32x16_bf16 v[96:111], a[252:255], a[60:63], v[96:111]\n\t"
    "ds_read_b128 a[252:255], v17 offset:2048\n\t"
    "v_exp_f32 v27, v27\n\t"
    "v_fma_f32 v26, v26, s77, s77\n\t"
    "v_rcp_f32 v35, v35\n\t"
    "s_waitcnt lgkmcnt(5)\n\t"
    "v_mfma_f32_32x32x16_bf16 v[96:111], v[48:51], a[64:67], v[96:111]\n\t"
    "v_mul_f32 v34, v184, v34\n\t"
    "v_mul_f32 v29, s76, v189\n\t"
    "v_exp_f32 v28, v28\n\t"
    "v_fma_f32 v27, v27, s77, s77\n\t"
    "v_mfma_f32_32x32x16_bf16 v[96:111], v[52:55], a[68:71], v[96:111]\n\t"
    "ds_read_b128 v[48:51], v17 offset:4096\n\t"
    "v_rcp_f32 v26, v26\n\t"
    "v_mul_f32 v35, v185, v35\n\t"
    "v_cvt_pk_bf16_f32 v184, v34, v35\n\t"
    "v_mul_f32 v30, s76, v190\n\t"
    "s_waitcnt lgkmcnt(4)\n\t"
    "v_mfma_f32_32x32x16_bf16 v[96:111], a[240:243], a[72:75], v[96:111]\n\t"
    "v_exp_f32 v29, v29\n\t"
    "v_fma_f32 v28, v28, s77, s77\n\t"
    "v_rcp_f32 v27, v27\n\t"
    "v_mfma_f32_32x32x16_bf16 v[96:111], a[244:247], a[76:79], v[96:111]\n\t"
    "ds_read_b128 v[52:55], v17 offset:6144\n\t"
    "v_mul_f32 v26, v186, v26\n\t"
    "v_mul_f32 v31, s76, v191\n\t"
    "v_exp_f32 v30, v30\n\t"
    "v_fma_f32 v29, v29, s77, s77\n\t"
    "s_waitcnt lgkmcnt(2)\n\t"
    "v_mfma_f32_32x32x16_bf16 v[112:127], a[248:251], a[0:3], v[112:127]\n\t"
    "v_rcp_f32 v28, v28\n\t"
    "v_mul_f32 v27, v187, v27\n\t"
    "v_cvt_pk_bf16_f32 v185, v26, v27\n\t"
    "v_mul_f32 v32, s76, v240\n\t"
    "v_mfma_f32_32x32x16_bf16 v[144:159], a[248:251], a[80:83], v[144:159]\n\t"
    "ds_read_b128 a[240:243], v17 offset:8192\n\t"
    "v_exp_f32 v31, v31\n\t"
    "v_fma_f32 v30, v30, s77, s77\n\t"
    "v_rcp_f32 v29, v29\n\t"
    "v_mul_f32 v28, v188, v28\n\t"
    "v_mfma_f32_32x32x16_bf16 v[112:127], a[252:255], a[4:7], v[112:127]\n\t"
    "v_cvt_pk_bf16_f32 v96, v96, v97\n\t"
    "v_cvt_pk_bf16_f32 v97, v98, v99\n\t"
    "v_cvt_pk_bf16_f32 v98, v100, v101\n\t"
    "v_cvt_pk_bf16_f32 v99, v102, v103\n\t"
    "v_mul_f32 v33, s76, v241\n\t"
    "v_exp_f32 v32, v32\n\t"
    "v_mfma_f32_32x32x16_bf16 v[144:159], a[252:255], a[84:87], v[144:159]\n\t"
    "ds_read_b128 a[244:247], v17 offset:10240\n\t"
    "v_cvt_pk_bf16_f32 v104, v104, v105\n\t"
    "v_cvt_pk_bf16_f32 v105, v106, v107\n\t"
    "v_fma_f32 v31, v31, s77, s77\n\t"
    "v_rcp_f32 v30, v30\n\t"
    "s_waitcnt lgkmcnt(2)\n\t"
    "v_mfma_f32_32x32x16_bf16 v[112:127], v[48:51], a[8:11], v[112:127]\n\t"
    "v_cvt_pk_bf16_f32 v106, v108, v109\n\t"
    "v_cvt_pk_bf16_f32 v107, v110, v111\n\t"
    "v_permlane16_swap_b32 v96, v104\n\t"
    ".Ledm_hop2_%=:\n\t"
    "s_cbranch_execz .Ledm_ret%=\n\t"
    "v_permlane16_swap_b32 v97, v105\n\t"
    "v_mul_f32 v29, v189, v29\n\t"
    "v_mfma_f32_32x32x16_bf16 v[144:159], v[48:51], a[88:91], v[144:159]\n\t"
    "ds_read_b128 a[248:251], v17 offset:12288\n\t"
    "v_permlane16_swap_b32 v98, v106\n\t"
    "v_permlane16_swap_b32 v99, v107\n\t"
    "v_cvt_pk_bf16_f32 v186, v28, v29\n\t"
    "v_mul_f32 v34, s76, v242\n\t"
    "v_exp_f32 v33, v33\n\t"
    "v_mfma_f32_32x32x16_bf16 v[112:127], v[52:55], a[12:15], v[112:127]\n\t"
    "global_store_dwordx4 v15, v[96:99], s[40:41] offset:0 sc0 sc1 nt\n\t"
    "v_fma_f32 v32, v32, s77, s77\n\t"
    "v_mfma_f32_32x32x16_bf16 v[144:159], v[52:55], a[92:95], v[144:159]\n\t"
    "ds_read_b128 a[252:255], v17 offset:14336\n\t"
    "v_rcp_f32 v31, v31\n\t"
    "v_mul_f32 v30, v190, v30\n\t"
    "v_mul_f32 v35, s76, v243\n\t"
    "s_waitcnt lgkmcnt(2)\n\t"
    "v_mfma_f32_32x32x16_bf16 v[112:127], a[240:243], a[16:19], v[112:127]\n\t"
    "v_exp_f32 v34, v34\n\t"
    "v_fma_f32 v33, v33, s77, s77\n\t"
    "v_rcp_f32 v32, v32\n\t"
    "v_mul_f32 v31, v191, v31\n\t"
    "v_mfma_f32_32x32x16_bf16 v[144:159], a[240:243], a[96:99], v[144:159]\n\t"
    "ds_read_b128 v[48:51], v17 offset:16384\n\t"
    "v_cvt_pk_bf16_f32 v187, v30, v31\n\t"
    "v_mfma_f32_32x32x16_bf16 v[112:127], a[244:247], a[20:23], v[112:127]\n\t"
    "global_store_dwordx4 v15, v[104:107], s[42:43] offset:0 sc0 sc1 nt\n\t"
    "ds_write_b128 v18, v[184:187] offset:2048\n\t"
    "v_mul_f32 v26, s76, v244\n\t"
    "v_mfma_f32_32x32x16_bf16 v[144:159], a[244:247], a[100:103], v[144:159]\n\t"
    "ds_read_b128 v[52:55], v17 offset:18432\n\t"
    "v_exp_f32 v35, v35\n\t"
    "v_fma_f32 v34, v34, s77, s77\n\t"
    "v_rcp_f32 v33, v33\n\t"
    "s_waitcnt lgkmcnt(3)\n\t"
    "v_mfma_f32_32x32x16_bf16 v[112:127], a[248:251], a[24:27], v[112:127]\n\t"
    "v_mul_f32 v32, v240, v32\n\t"
    "v_mul_f32 v27, s76, v245\n\t"
    "v_exp_f32 v26, v26\n\t"
    "v_fma_f32 v35, v35, s77, s77\n\t"
    "v_rcp_f32 v34, v34\n\t"
    "v_mfma_f32_32x32x16_bf16 v[144:159], a[248:251], a[104:107], v[144:159]\n\t"
    "ds_read_b128 a[240:243], v17 offset:20480\n\t"
    "v_mul_f32 v33, v241, v33\n\t"
    "v_cvt_pk_bf16_f32 v240, v32, v33\n\t"
    "v_mul_f32 v28, s76, v246\n\t"
    "v_exp_f32 v27, v27\n\t"
    "v_mfma_f32_32x32x16_bf16 v[112:127], a[252:255], a[28:31], v[112:127]\n\t"
    "v_fma_f32 v26, v26, s77, s77\n\t"
    "v_rcp_f32 v35, v35\n\t"
    "v_mul_f32 v34, v242, v34\n\t"
    "v_mul_f32 v29, s76, v247\n\t"
    "v_mfma_f32_32x32x16_bf16 v[144:159], a[252:255], a[108:111], v[144:159]\n\t"
    "ds_read_b128 a[244:247], v17 offset:22528\n\t"
    "v_exp_f32 v28, v28\n\t"
    "v_fma_f32 v27, v27, s77, s77\n\t"
    "v_rcp_f32 v26, v26\n\t"
    "v_mul_f32 v35, v243, v35\n\t"
    "s_waitcnt lgkmcnt(2)\n\t"
    "v_mfma_f32_32x32x16_bf16 v[112:127], v[48:51], a[32:35], v[112:127]\n\t"
    "v_cvt_pk_bf16_f32 v241, v34, v35\n\t"
    "v_mul_f32 v30, s76, v248\n\t"
    "v_exp_f32 v29, v29\n\t"
    "v_fma_f32 v28, v28, s77, s77\n\t"
    "v_mfma_f32_32x32x16_bf16 v[144:159], v[48:51], a[112:115], v[144:159]\n\t"
    "ds_read_b128 a[248:251], v17 offset:24576\n\t"
    "v_rcp_f32 v27, v27\n\t"
    "v_mul_f32 v26, v244, v26\n\t"
    "v_mul_f32 v31, s76, v249\n\t"
    "v_exp_f32 v30, v30\n\t"
    "v_mfma_f32_32x32x16_bf16 v[112:127], v[52:55], a[36:39], v[112:127]\n\t"
    "v_fma_f32 v29, v29, s77, s77\n\t"
    "v_rcp_f32 v28, v28\n\t"
    "v_mul_f32 v27, v245, v27\n\t"
    "v_cvt_pk_bf16_f32 v242, v26, v27\n\t"
    "v_mul_f32 v32, s76, v250\n\t"
    "v_mfma_f32_32x32x16_bf16 v[144:159], v[52:55], a[116:119], v[144:159]\n\t"
    "ds_read_b128 a[252:255], v17 offset:26624\n\t"
    "v_exp_f32 v31, v31\n\t"
    "v_fma_f32 v30, v30, s77, s77\n\t"
    "v_rcp_f32 v29, v29\n\t"
    "s_waitcnt lgkmcnt(2)\n\t"
    "v_mfma_f32_32x32x16_bf16 v[112:127], a[240:243], a[40:43], v[112:127]\n\t"
    "v_mul_f32 v28, v246, v28\n\t"
    "v_mul_f32 v33, s76, v251\n\t"
    "v_exp_f32 v32, v32\n\t"
    "v_fma_f32 v31, v31, s77, s77\n\t"
    "v_mfma_f32_32x32x16_bf16 v[144:159], a[240:243], a[120:123], v[144:159]\n\t"
    "ds_read_b128 v[48:51], v17 offset:28672\n\t"
    "v_rcp_f32 v30, v30\n\t"
    "v_mul_f32 v29, v247, v29\n\t"
    "v_cvt_pk_bf16_f32 v243, v28, v29\n\t"
    "v_mfma_f32_32x32x16_bf16 v[112:127], a[244:247], a[44:47], v[112:127]\n\t"
    "ds_write_b128 v20, v[240:243] offset:0\n\t"
    "v_mul_f32 v34, s76, v252\n\t"
    "v_exp_f32 v33, v33\n\t"
    "v_fma_f32 v32, v32, s77, s77\n\t"
    "v_mfma_f32_32x32x16_bf16 v[144:159], a[244:247], a[124:127], v[144:159]\n\t"
    "ds_read_b128 v[52:55], v17 offset:30720\n\t"
    "v_rcp_f32 v31, v31\n\t"
    "v_mul_f32 v30, v248, v30\n\t"
    "v_mul_f32 v35, s76, v253\n\t"
    "s_waitcnt lgkmcnt(3)\n\t"
    "v_mfma_f32_32x32x16_bf16 v[112:127], a[248:251], a[48:51], v[112:127]\n\t"
    "v_exp_f32 v34, v34\n\t"
    "v_fma_f32 v33, v33, s77, s77\n\t"
    "v_rcp_f32 v32, v32\n\t"
    "v_mul_f32 v31, v249, v31\n\t"
    "v_cvt_pk_bf16_f32 v248, v30, v31\n\t"
    "v_mfma_f32_32x32x16_bf16 v[144:159], a[248:251], a[128:131], v[144:159]\n\t"
    "ds_read_b128 a[240:243], v17 offset:32768\n\t"
    "v_mul_f32 v26, s76, v254\n\t"
    "v_exp_f32 v35, v35\n\t"
    "v_fma_f32 v34, v34, s77, s77\n\t"
    "v_mfma_f32_32x32x16_bf16 v[112:127], a[252:255], a[52:55], v[112:127]\n\t"
    "v_rcp_f32 v33, v33\n\t"
    "v_mul_f32 v32, v250, v32\n\t"
    "v_mul_f32 v27, s76, v255\n\t"
    "v_exp_f32 v26, v26\n\t"
    "v_fma_f32 v35, v35, s77, s77\n\t"
    "v_mfma_f32_32x32x16_bf16 v[144:159], a[252:255], a[132:135], v[144:159]\n\t"
    "ds_read_b128 a[244:247], v17 offset:34816\n\t"
    "v_rcp_f32 v34, v34\n\t"
    "v_mul_f32 v33, v251, v33\n\t"
    "v_cvt_pk_bf16_f32 v249, v32, v33\n\t"
    "v_mul_f32 v28, s76, v192\n\t"
    "s_waitcnt lgkmcnt(2)\n\t"
    "v_mfma_f32_32x32x16_bf16 v[112:127], v[48:51], a[56:59], v[112:127]\n\t"
    "v_exp_f32 v27, v27\n\t"
    "v_fma_f32 v26, v26, s77, s77\n\t"
    "v_rcp_f32 v35, v35\n\t"
    "v_mul_f32 v34, v252, v34\n\t"
    "v_mfma_f32_32x32x16_bf16 v[144:159], v[48:51], a[136:139], v[144:159]\n\t"
    "ds_read_b128 a[248:251], v17 offset:36864\n\t"
    "v_mul_f32 v29, s76, v193\n\t"
    "v_exp_f32 v28, v28\n\t"
    "v_fma_f32 v27, v27, s77, s77\n\t"
    "v_mfma_f32_32x32x16_bf16 v[112:127], v[52:55], a[60:63], v[112:127]\n\t"
    "v_rcp_f32 v26, v26\n\t"
    "v_mul_f32 v35, v253, v35\n\t"
    "v_cvt_pk_bf16_f32 v250, v34, v35\n\t"
    "v_mul_f32 v30, s76, v194\n\t"
    "v_exp_f32 v29, v29\n\t"
    "v_mfma_f32_32x32x16_bf16 v[144:159], v[52:55], a[140:143], v[144:159]\n\t"
    "ds_read_b128 a[252:255], v17 offset:38912\n\t"
    "v_fma_f32 v28, v28, s77, s77\n\t"
    "v_rcp_f32 v27, v27\n\t"
    "v_mul_f32 v26, v254, v26\n\t"
    "v_mul_f32 v31, s76, v195\n\t"
    "s_waitcnt lgkmcnt(2)\n\t"
    "v_mfma_f32_32x32x16_bf16 v[112:127], a[240:243], a[64:67], v[112:127]\n\t"
    "v_exp_f32 v30, v30\n\t"
    "v_fma_f32 v29, v29, s77, s77\n\t"
    "v_rcp_f32 v28, v28\n\t"
    "v_mul_f32 v27, v255, v27\n\t"
    "v_mfma_f32_32x32x16_bf16 v[144:159], a[240:243], a[144:147], v[144:159]\n\t"
    "ds_read_b128 v[48:51], v16 offset:0\n\t"
    "v_cvt_pk_bf16_f32 v251, v26, v27\n\t"
    "v_mfma_f32_32x32x16_bf16 v[112:127], a[244:247], a[68:71], v[112:127]\n\t"
    "ds_write_b128 v20, v[248:251] offset:2048\n\t"
    "v_mul_f32 v32, s76, v196\n\t"
    "v_exp_f32 v31, v31\n\t"
    "v_fma_f32 v30, v30, s77, s77\n\t"
    "v_rcp_f32 v29, v29\n\t"
    "v_mfma_f32_32x32x16_bf16 v[144:159], a[244:247], a[148:151], v[144:159]\n\t"
    "ds_read_b128 v[52:55], v16 offset:2048\n\t"
    "v_mul_f32 v28, v192, v28\n\t"
    "v_mul_f32 v33, s76, v197\n\t"
    "v_exp_f32 v32, v32\n\t"
    "v_fma_f32 v31, v31, s77, s77\n\t"
    "s_waitcnt lgkmcnt(3)\n\t"
    "v_mfma_f32_32x32x16_bf16 v[112:127], a[248:251], a[72:75], v[112:127]\n\t"
    "v_rcp_f32 v30, v30\n\t"
    "v_mul_f32 v29, v193, v29\n\t"
    "v_cvt_pk_bf16_f32 v192, v28, v29\n\t"
    "v_mul_f32 v34, s76, v198\n\t"
    "v_mfma_f32_32x32x16_bf16 v[144:159], a[248:251], a[152:155], v[144:159]\n\t"
    "ds_read_b128 a[240:243], v16 offset:4096\n\t"
    "v_exp_f32 v33, v33\n\t"
    "v_fma_f32 v32, v32, s77, s77\n\t"
    "v_rcp_f32 v31, v31\n\t"
    "v_mul_f32 v30, v194, v30\n\t"
    "v_mfma_f32_32x32x16_bf16 v[112:127], a[252:255], a[76:79], v[112:127]\n\t"
    "v_mul_f32 v35, s76, v199\n\t"
    "v_exp_f32 v34, v34\n\t"
    "v_fma_f32 v33, v33, s77, s77\n\t"
    "v_rcp_f32 v32, v32\n\t"
    "v_mfma_f32_32x32x16_bf16 v[144:159], a[252:255], a[156:159], v[144:159]\n\t"
    "ds_read_b128 a[244:247], v16 offset:6144\n\t"
    "v_mul_f32 v31, v195, v31\n\t"
    "v_cvt_pk_bf16_f32 v193, v30, v31\n\t"
    "v_mul_f32 v26, s76, v200\n\t"
    "v_exp_f32 v35, v35\n\t"
    "s_waitcnt lgkmcnt(2)\n\t"
    "v_mfma_f32_32x32x16_bf16 v[128:143], v[48:51], a[80:83], v[128:143]\n\t"
    "v_fma_f32 v34, v34, s77, s77\n\t"
    "v_rcp_f32 v33, v33\n\t"
    "v_mul_f32 v32, v196, v32\n\t"
    "v_mul_f32 v27, s76, v201\n\t"
    "v_exp_f32 v26, v26\n\t"
    "v_mfma_f32_32x32x16_bf16 v[160:175], v[48:51], a[160:163], v[160:175]\n\t"
    "ds_read_b128 a[248:251], v16 offset:8192\n\t"
    "v_cvt_pk_bf16_f32 v112, v112, v113\n\t"
    "v_cvt_pk_bf16_f32 v113, v114, v115\n\t"
    "v_fma_f32 v35, v35, s77, s77\n\t"
    "v_rcp_f32 v34, v34\n\t"
    "v_mfma_f32_32x32x16_bf16 v[128:143], v[52:55], a[84:87], v[128:143]\n\t"
    "v_cvt_pk_bf16_f32 v114, v116, v117\n\t"
    "v_cvt_pk_bf16_f32 v115, v118, v119\n\t"
    "v_cvt_pk_bf16_f32 v120, v120, v121\n\t"
    "v_cvt_pk_bf16_f32 v121, v122, v123\n\t"
    "v_cvt_pk_bf16_f32 v144, v144, v145\n\t"
    "v_cvt_pk_bf16_f32 v145, v146, v147\n\t"
    "v_cvt_pk_bf16_f32 v146, v148, v149\n\t"
    "v_cvt_pk_bf16_f32 v147, v150, v151\n\t"
    "v_mfma_f32_32x32x16_bf16 v[160:175], v[52:55], a[164:167], v[160:175]\n\t"
    "ds_read_b128 a[252:255], v16 offset:10240\n\t"
    "v_cvt_pk_bf16_f32 v122, v124, v125\n\t"
    "v_cvt_pk_bf16_f32 v123, v126, v127\n\t"
    "v_permlane16_swap_b32 v112, v120\n\t"
    "v_cvt_pk_bf16_f32 v152, v152, v153\n\t"
    "v_cvt_pk_bf16_f32 v153, v154, v155\n\t"
    "v_cvt_pk_bf16_f32 v154, v156, v157\n\t"
    "s_waitcnt lgkmcnt(2)\n\t"
    "v_mfma_f32_32x32x16_bf16 v[128:143], a[240:243], a[88:91], v[128:143]\n\t"
    "v_permlane16_swap_b32 v113, v121\n\t"
    "v_permlane16_swap_b32 v114, v122\n\t"
    "v_permlane16_swap_b32 v115, v123\n\t"
    "v_cvt_pk_bf16_f32 v155, v158, v159\n\t"
    "v_permlane16_swap_b32 v144, v152\n\t"
    "v_permlane16_swap_b32 v145, v153\n\t"
    "v_permlane16_swap_b32 v146, v154\n\t"
    "v_mfma_f32_32x32x16_bf16 v[160:175], a[240:243], a[168:171], v[160:175]\n\t"
    "ds_read_b128 v[48:51], v16 offset:12288\n\t"
    "global_store_dwordx4 v25, v[112:115], s[40:41] offset:0 sc0 sc1 nt\n\t"
    "v_permlane16_swap_b32 v147, v155\n\t"
    "v_mfma_f32_32x32x16_bf16 v[128:143], a[244:247], a[92:95], v[128:143]\n\t"
    "global_store_dwordx4 v25, v[144:147], s[44:45] offset:0 sc0 sc1 nt\n\t"
    "v_mul_f32 v33, v197, v33\n\t"
    "v_mfma_f32_32x32x16_bf16 v[160:175], a[244:247], a[172:175], v[160:175]\n\t"
    "ds_read_b128 v[52:55], v16 offset:14336\n\t"
    "v_cvt_pk_bf16_f32 v194, v32, v33\n\t"
    "v_mul_f32 v28, s76, v202\n\t"
    "v_exp_f32 v27, v27\n\t"
    "v_fma_f32 v26, v26, s77, s77\n\t"
    "s_waitcnt lgkmcnt(2)\n\t"
    "v_mfma_f32_32x32x16_bf16 v[128:143], a[248:251], a[96:99], v[128:143]\n\t"
    "v_rcp_f32 v35, v35\n\t"
    "v_mul_f32 v34, v198, v34\n\t"
    "v_mul_f32 v29, s76, v203\n\t"
    "v_exp_f32 v28, v28\n\t"
    "v_mfma_f32_32x32x16_bf16 v[160:175], a[248:251], a[176:179], v[160:175]\n\t"
    "ds_read_b128 a[240:243], v16 offset:16384\n\t"
    "global_store_dwordx4 v25, v[120:123], s[42:43] offset:0 sc0 sc1 nt\n\t"
    "v_mfma_f32_32x32x16_bf16 v[128:143], a[252:255], a[100:103], v[128:143]\n\t"
    "global_store_dwordx4 v25, v[152:155], s[46:47] offset:0 sc0 sc1 nt\n\t"
    "v_fma_f32 v27, v27, s77, s77\n\t"
    "v_mfma_f32_32x32x16_bf16 v[160:175], a[252:255], a[180:183], v[160:175]\n\t"
    "ds_read_b128 a[244:247], v16 offset:18432\n\t"
    "v_rcp_f32 v26, v26\n\t"
    "v_mul_f32 v35, v199, v35\n\t"
    "v_cvt_pk_bf16_f32 v195, v34, v35\n\t"
    "s_waitcnt lgkmcnt(2)\n\t"
    "v_mfma_f32_32x32x16_bf16 v[128:143], v[48:51], a[104:107], v[128:143]\n\t"
    "ds_write_b128 v19, v[192:195] offset:0\n\t"
    "v_mul_f32 v30, s76, v204\n\t"
    "v_exp_f32 v29, v29\n\t"
    "v_mfma_f32_32x32x16_bf16 v[160:175], v[48:51], a[184:187], v[160:175]\n\t"
    "ds_read_b128 a[248:251], v16 offset:20480\n\t"
    "v_fma_f32 v28, v28, s77, s77\n\t"
    "v_rcp_f32 v27, v27\n\t"
    "v_mul_f32 v26, v200, v26\n\t"
    "v_mfma_f32_32x32x16_bf16 v[128:143], v[52:55], a[108:111], v[128:143]\n\t"
    "v_mul_f32 v31, s76, v205\n\t"
    "v_exp_f32 v30, v30\n\t"
    "v_fma_f32 v29, v29, s77, s77\n\t"
    "v_rcp_f32 v28, v28\n\t"
    "v_mul_f32 v27, v201, v27\n\t"
    "v_mfma_f32_32x32x16_bf16 v[160:175], v[52:55], a[188:191], v[160:175]\n\t"
    "ds_read_b128 a[252:255], v16 offset:22528\n\t"
    "v_cvt_pk_bf16_f32 v200, v26, v27\n\t"
    "v_mul_f32 v32, s76, v206\n\t"
    "v_exp_f32 v31, v31\n\t"
    "v_fma_f32 v30, v30, s77, s77\n\t"
    "s_waitcnt lgkmcnt(3)\n\t"
    "v_mfma_f32_32x32x16_bf16 v[128:143], a[240:243], a[112:115], v[128:143]\n\t"
    "v_rcp_f32 v29, v29\n\t"
    "v_mul_f32 v28, v202, v28\n\t"
    "v_mul_f32 v33, s76, v207\n\t"
    "v_exp_f32 v32, v32\n\t"
    "v_mfma_f32_32x32x16_bf16 v[160:175], a[240:243], a[192:195], v[160:175]\n\t"
    "ds_read_b128 v[48:51], v16 offset:24576\n\t"
    "v_fma_f32 v31, v31, s77, s77\n\t"
    "v_rcp_f32 v30, v30\n\t"
    "v_mul_f32 v29, v203, v29\n\t"
    "v_cvt_pk_bf16_f32 v201, v28, v29\n\t"
    "v_mfma_f32_32x32x16_bf16 v[128:143], a[244:247], a[116:119], v[128:143]\n\t"
    "v_mul_f32 v34, s76, v208\n\t"
    "v_exp_f32 v33, v33\n\t"
    "v_fma_f32 v32, v32, s77, s77\n\t"
    "v_rcp_f32 v31, v31\n\t"
    "v_mfma_f32_32x32x16_bf16 v[160:175], a[244:247], a[196:199], v[160:175]\n\t"
    "ds_read_b128 v[52:55], v16 offset:26624\n\t"
    "v_mul_f32 v30, v204, v30\n\t"
    "v_mul_f32 v35, s76, v209\n\t"
    "v_exp_f32 v34, v34\n\t"
    "v_fma_f32 v33, v33, s77, s77\n\t"
    "s_waitcnt lgkmcnt(2)\n\t"
    "v_mfma_f32_32x32x16_bf16 v[128:143], a[248:251], a[120:123], v[128:143]\n\t"
    "v_rcp_f32 v32, v32\n\t"
    "v_mul_f32 v31, v205, v31\n\t"
    "v_cvt_pk_bf16_f32 v202, v30, v31\n\t"
    "v_mul_f32 v26, s76, v210\n\t"
    "v_mfma_f32_32x32x16_bf16 v[160:175], a[248:251], a[200:203], v[160:175]\n\t"
    "ds_read_b128 a[240:243], v16 offset:28672\n\t"
    "v_exp_f32 v35, v35\n\t"
    "v_fma_f32 v34, v34, s77, s77\n\t"
    "v_rcp_f32 v33, v33\n\t"
    "v_mul_f32 v32, v206, v32\n\t"
    "v_mfma_f32_32x32x16_bf16 v[128:143], a[252:255], a[124:127], v[128:143]\n\t"
    "v_mul_f32 v27, s76, v211\n\t"
    "v_exp_f32 v26, v26\n\t"
    "v_fma_f32 v35, v35, s77, s77\n\t"
    "v_rcp_f32 v34, v34\n\t"
    "v_mfma_f32_32x32x16_bf16 v[160:175], a[252:255], a[204:207], v[160:175]\n\t"
    "ds_read_b128 a[244:247], v16 offset:30720\n\t"
    "v_mul_f32 v33, v207, v33\n\t"
    "v_cvt_pk_bf16_f32 v203, v32, v33\n\t"
    "s_waitcnt lgkmcnt(2)\n\t"
    "v_mfma_f32_32x32x16_bf16 v[128:143], v[48:51], a[128:131], v[128:143]\n\t"
    "ds_write_b128 v19, v[200:203] offset:2048\n\t"
    "v_mul_f32 v28, s76, v212\n\t"
    "v_exp_f32 v27, v27\n\t"
    "v_fma_f32 v26, v26, s77, s77\n\t"
    "v_mfma_f32_32x32x16_bf16 v[160:175], v[48:51], a[208:211], v[160:175]\n\t"
    "ds_read_b128 a[248:251], v16 offset:32768\n\t"
    "v_rcp_f32 v35, v35\n\t"
    "v_mul_f32 v34, v208, v34\n\t"
    "v_mul_f32 v29, s76, v213\n\t"
    "v_exp_f32 v28, v28\n\t"
    "v_mfma_f32_32x32x16_bf16 v[128:143], v[52:55], a[132:135], v[128:143]\n\t"
    "v_fma_f32 v27, v27, s77, s77\n\t"
    "v_rcp_f32 v26, v26\n\t"
    "v_mul_f32 v35, v209, v35\n\t"
    "v_cvt_pk_bf16_f32 v208, v34, v35\n\t"
    "v_mul_f32 v30, s76, v214\n\t"
    "v_mfma_f32_32x32x16_bf16 v[160:175], v[52:55], a[212:215], v[160:175]\n\t"
    "ds_read_b128 a[252:255], v16 offset:34816\n\t"
    "v_exp_f32 v29, v29\n\t"
    "v_fma_f32 v28, v28, s77, s77\n\t"
    "v_rcp_f32 v27, v27\n\t"
    "s_waitcnt lgkmcnt(3)\n\t"
    "v_mfma_f32_32x32x16_bf16 v[128:143], a[240:243], a[136:139], v[128:143]\n\t"
    "v_mul_f32 v26, v210, v26\n\t"
    "v_mul_f32 v31, s76, v215\n\t"
    "v_exp_f32 v30, v30\n\t"
    "v_fma_f32 v29, v29, s77, s77\n\t"
    "v_mfma_f32_32x32x16_bf16 v[160:175], a[240:243], a[216:219], v[160:175]\n\t"
    "ds_read_b128 v[48:51], v16 offset:36864\n\t"
    "v_rcp_f32 v28, v28\n\t"
    "v_mul_f32 v27, v211, v27\n\t"
    "v_cvt_pk_bf16_f32 v209, v26, v27\n\t"
    "v_mul_f32 v32, s76, v216\n\t"
    "v_mfma_f32_32x32x16_bf16 v[128:143], a[244:247], a[140:143], v[128:143]\n\t"
    "v_exp_f32 v31, v31\n\t"
    "v_fma_f32 v30, v30, s77, s77\n\t"
    "v_rcp_f32 v29, v29\n\t"
    "v_mul_f32 v28, v212, v28\n\t"
    "v_mul_f32 v33, s76, v217\n\t"
    "v_mfma_f32_32x32x16_bf16 v[160:175], a[244:247], a[220:223], v[160:175]\n\t"
    "ds_read_b128 v[52:55], v16 offset:38912\n\t"
    "v_exp_f32 v32, v32\n\t"
    "v_fma_f32 v31, v31, s77, s77\n\t"
    "v_rcp_f32 v30, v30\n\t"
    "s_waitcnt lgkmcnt(2)\n\t"
    "v_mfma_f32_32x32x16_bf16 v[128:143], a[248:251], a[144:147], v[128:143]\n\t"
    "v_mul_f32 v29, v213, v29\n\t"
    "v_cvt_pk_bf16_f32 v210, v28, v29\n\t"
    "v_mul_f32 v34, s76, v218\n\t"
    "v_exp_f32 v33, v33\n\t"
    "v_fma_f32 v32, v32, s77, s77\n\t"
    "v_mfma_f32_32x32x16_bf16 v[160:175], a[248:251], a[224:227], v[160:175]\n\t"
    "v_rcp_f32 v31, v31\n\t"
    "v_mul_f32 v30, v214, v30\n\t"
    "v_mul_f32 v35, s76, v219\n\t"
    "v_exp_f32 v34, v34\n\t"
    "v_mfma_f32_32x32x16_bf16 v[128:143], a[252:255], a[148:151], v[128:143]\n\t"
    "v_fma_f32 v33, v33, s77, s77\n\t"
    "v_rcp_f32 v32, v32\n\t"
    "v_mul_f32 v31, v215, v31\n\t"
    "v_cvt_pk_bf16_f32 v211, v30, v31\n\t"
    "v_mfma_f32_32x32x16_bf16 v[160:175], a[252:255], a[228:231], v[160:175]\n\t"
    "ds_write_b128 v18, v[208:211] offset:16384\n\t"
    "v_mul_f32 v26, s76, v220\n\t"
    "v_exp_f32 v35, v35\n\t"
    "s_waitcnt lgkmcnt(1)\n\t"
    "v_mfma_f32_32x32x16_bf16 v[128:143], v[48:51], a[152:155], v[128:143]\n\t"
    "v_fma_f32 v34, v34, s77, s77\n\t"
    "v_rcp_f32 v33, v33\n\t"
    "v_mul_f32 v32, v216, v32\n\t"
    "v_mul_f32 v27, s76, v221\n\t"
    "v_exp_f32 v26, v26\n\t"
    "v_mfma_f32_32x32x16_bf16 v[160:175], v[48:51], a[232:235], v[160:175]\n\t"
    "v_fma_f32 v35, v35, s77, s77\n\t"
    "v_rcp_f32 v34, v34\n\t"
    "v_mul_f32 v33, v217, v33\n\t"
    "v_cvt_pk_bf16_f32 v216, v32, v33\n\t"
    "v_mfma_f32_32x32x16_bf16 v[128:143], v[52:55], a[156:159], v[128:143]\n\t"
    "v_mfma_f32_32x32x16_bf16 v[160:175], v[52:55], a[236:239], v[160:175]\n\t"
    "v_subrev_u32 v16, 122880, v16\n\t"
    "v_subrev_u32 v17, 122880, v17\n\t"
    "s_barrier\n\t"
    "v_mfma_f32_32x32x16_bf16 v[176:191], v[56:59], v[36:39], 0\n\t"
    "ds_read_b128 a[240:243], v16 offset:0\n\t"
    "v_mul_f32 v28, s76, v222\n\t"
    "v_exp_f32 v27, v27\n\t"
    "v_fma_f32 v26, v26, s77, s77\n\t"
    "v_rcp_f32 v35, v35\n\t"
    "v_mfma_f32_32x32x16_bf16 v[176:191], v[60:63], v[40:43], v[176:191]\n\t"
    "ds_read_b128 a[244:247], v16 offset:2048\n\t"
    "v_mul_f32 v34, v218, v34\n\t"
    "v_mul_f32 v29, s76, v223\n\t"
    "v_exp_f32 v28, v28\n\t"
    "v_fma_f32 v27, v27, s77, s77\n\t"
    "v_rcp_f32 v26, v26\n\t"
    "v_mul_f32 v35, v219, v35\n\t"
    "v_mfma_f32_32x32x16_bf16 v[240:255], v[88:91], v[36:39], 0\n\t"
    "v_cvt_pk_bf16_f32 v128, v128, v129\n\t"
    "v_cvt_pk_bf16_f32 v129, v130, v131\n\t"
    "v_cvt_pk_bf16_f32 v130, v132, v133\n\t"
    "v_cvt_pk_bf16_f32 v131, v134, v135\n\t"
    "v_cvt_pk_bf16_f32 v217, v34, v35\n\t"
    "v_mul_f32 v30, s76, v224\n\t"
    "v_exp_f32 v29, v29\n\t"
    "v_fma_f32 v28, v28, s77, s77\n\t"
    "v_mfma_f32_32x32x16_bf16 v[240:255], v[92:95], v[40:43], v[240:255]\n\t"
    "ds_read_b128 a[248:251], v16 offset:4096\n\t"
    "v_cvt_pk_bf16_f32 v136, v136, v137\n\t"
    "v_cvt_pk_bf16_f32 v137, v138, v139\n\t"
    "v_rcp_f32 v27, v27\n\t"
    "v_mul_f32 v26, v220, v26\n\t"
    "v_mul_f32 v31, s76, v225\n\t"
    "v_exp_f32 v30, v30\n\t"
    "v_mfma_f32_32x32x16_bf16 v[192:207], v[64:67], v[36:39], 0\n\t"
    "v_cvt_pk_bf16_f32 v138, v140, v141\n\t"
    "v_cvt_pk_bf16_f32 v139, v142, v143\n\t"
    "v_permlane16_swap_b32 v128, v136\n\t"
    "v_permlane16_swap_b32 v129, v137\n\t"
    "v_fma_f32 v29, v29, s77, s77\n\t"
    "v_rcp_f32 v28, v28\n\t"
    "v_mul_f32 v27, v221, v27\n\t"
    "v_cvt_pk_bf16_f32 v218, v26, v27\n\t"
    "v_mfma_f32_32x32x16_bf16 v[192:207], v[68:71], v[40:43], v[192:207]\n\t"
    "ds_read_b128 a[252:255], v16 offset:6144\n\t"
    "v_permlane16_swap_b32 v130, v138\n\t"
    "v_permlane16_swap_b32 v131, v139\n\t"
    "v_mul_f32 v32, s76, v226\n\t"
    "v_exp_f32 v31, v31\n\t"
    "v_fma_f32 v30, v30, s77, s77\n\t"
    "v_rcp_f32 v29, v29\n\t"
    "s_waitcnt lgkmcnt(2)\n\t"
    "v_mfma_f32_32x32x16_bf16 v[176:191], a[240:243], a[0:3], v[176:191]\n\t"
    "global_store_dwordx4 v15, v[128:131], s[44:45] offset:0 sc0 sc1 nt\n\t"
    "v_mul_f32 v28, v222, v28\n\t"
    "v_mul_f32 v33, s76, v227\n\t"
    "v_exp_f32 v32, v32\n\t"
    "v_mfma_f32_32x32x16_bf16 v[240:255], a[240:243], a[160:163], v[240:255]\n\t"
    "ds_read_b128 v[48:51], v16 offset:8192\n\t"
    "v_fma_f32 v31, v31, s77, s77\n\t"
    "v_rcp_f32 v30, v30\n\t"
    "v_mul_f32 v29, v223, v29\n\t"
    "v_cvt_pk_bf16_f32 v219, v28, v29\n\t"
    "v_mfma_f32_32x32x16_bf16 v[176:191], a[244:247], a[4:7], v[176:191]\n\t"
    "ds_write_b128 v18, v[216:219] offset:18432\n\t"
    "v_mul_f32 v34, s76, v228\n\t"
    "v_exp_f32 v33, v33\n\t"
    "v_fma_f32 v32, v32, s77, s77\n\t"
    "v_rcp_f32 v31, v31\n\t"
    "v_mul_f32 v30, v224, v30\n\t"
    "v_mfma_f32_32x32x16_bf16 v[240:255], a[244:247], a[164:167], v[240:255]\n\t"
    "ds_read_b128 v[52:55], v16 offset:10240\n\t"
    "v_mul_f32 v35, s76, v229\n\t"
    "v_exp_f32 v34, v34\n\t"
    "v_fma_f32 v33, v33, s77, s77\n\t"
    "v_rcp_f32 v32, v32\n\t"
    "v_mul_f32 v31, v225, v31\n\t"
    "v_cvt_pk_bf16_f32 v224, v30, v31\n\t"
    "s_waitcnt lgkmcnt(3)\n\t"
    "v_mfma_f32_32x32x16_bf16 v[176:191], a[248:251], a[8:11], v[176:191]\n\t"
    "global_store_dwordx4 v15, v[136:139], s[46:47] offset:0 sc0 sc1 nt\n\t"
    "v_mul_f32 v26, s76, v230\n\t"
    "v_exp_f32 v35, v35\n\t"
    "v_fma_f32 v34, v34, s77, s77\n\t"
    "v_mfma_f32_32x32x16_bf16 v[240:255], a[248:251], a[168:171], v[240:255]\n\t"
    "ds_read_b128 a[240:243], v16 offset:12288\n\t"
    "v_rcp_f32 v33, v33\n\t"
    "v_mul_f32 v32, v226, v32\n\t"
    "v_mul_f32 v27, s76, v231\n\t"
    "v_exp_f32 v26, v26\n\t"
    "v_fma_f32 v35, v35, s77, s77\n\t"
    "v_mfma_f32_32x32x16_bf16 v[176:191], a[252:255], a[12:15], v[176:191]\n\t"
    "v_rcp_f32 v34, v34\n\t"
    "v_mul_f32 v33, v227, v33\n\t"
    "v_cvt_pk_bf16_f32 v225, v32, v33\n\t"
    "v_mul_f32 v28, s76, v232\n\t"
    "v_exp_f32 v27, v27\n\t"
    "v_fma_f32 v26, v26, s77, s77\n\t"
    "v_rcp_f32 v35, v35\n\t"
    "v_mfma_f32_32x32x16_bf16 v[240:255], a[252:255], a[172:175], v[240:255]\n\t"
    "ds_read_b128 a[244:247], v16 offset:14336\n\t"
    "v_mul_f32 v34, v228, v34\n\t"
    "v_mul_f32 v29, s76, v233\n\t"
    "v_exp_f32 v28, v28\n\t"
    "v_fma_f32 v27, v27, s77, s77\n\t"
    "v_rcp_f32 v26, v26\n\t"
    "v_mul_f32 v35, v229, v35\n\t"
    "s_waitcnt lgkmcnt(2)\n\t"
    "v_mfma_f32_32x32x16_bf16 v[176:191], v[48:51], a[16:19], v[176:191]\n\t"
    "v_cvt_pk_bf16_f32 v160, v160, v161\n\t"
    "v_cvt_pk_bf16_f32 v161, v162, v163\n\t"
    "v_cvt_pk_bf16_f32 v162, v164, v165\n\t"
    "v_cvt_pk_bf16_f32 v226, v34, v35\n\t"
    "v_mul_f32 v30, s76, v234\n\t"
    "v_exp_f32 v29, v29\n\t"
    "v_fma_f32 v28, v28, s77, s77\n\t"
    "v_mfma_f32_32x32x16_bf16 v[240:255], v[48:51], a[176:179], v[240:255]\n\t"
    "ds_read_b128 a[248:251], v16 offset:32768\n\t"
    "v_cvt_pk_bf16_f32 v163, v166, v167\n\t"
    "v_cvt_pk_bf16_f32 v168, v168, v169\n\t"
    "v_cvt_pk_bf16_f32 v169, v170, v171\n\t"
    "v_rcp_f32 v27, v27\n\t"
    "v_mul_f32 v26, v230, v26\n\t"
    "v_mul_f32 v31, s76, v235\n\t"
    "v_mfma_f32_32x32x16_bf16 v[176:191], v[52:55], a[20:23], v[176:191]\n\t"
    "v_cvt_pk_bf16_f32 v170, v172, v173\n\t"
    "v_cvt_pk_bf16_f32 v171, v174, v175\n\t"
    "v_permlane16_swap_b32 v160, v168\n\t"
    "v_exp_f32 v30, v30\n\t"
    "v_fma_f32 v29, v29, s77, s77\n\t"
    "v_rcp_f32 v28, v28\n\t"
    "v_mul_f32 v27, v231, v27\n\t"
    "v_cvt_pk_bf16_f32 v227, v26, v27\n\t"
    "v_mfma_f32_32x32x16_bf16 v[240:255], v[52:55], a[180:183], v[240:255]\n\t"
    "ds_read_b128 a[252:255], v16 offset:34816\n\t"
    "ds_write_b128 v19, v[224:227] offset:16384\n\t"
    "v_permlane16_swap_b32 v161, v169\n\t"
    "v_permlane16_swap_b32 v162, v170\n\t"
    "v_permlane16_swap_b32 v163, v171\n\t"
    "v_mul_f32 v32, s76, v236\n\t"
    "s_waitcnt lgkmcnt(3)\n\t"
    "v_mfma_f32_32x32x16_bf16 v[176:191], a[240:243], a[24:27], v[176:191]\n\t"
    "global_store_dwordx4 v15, v[160:163], s[48:49] offset:0 sc0 sc1 nt\n\t"
    "v_exp_f32 v31, v31\n\t"
    "v_fma_f32 v30, v30, s77, s77\n\t"
    "v_mfma_f32_32x32x16_bf16 v[240:255], a[240:243], a[184:187], v[240:255]\n\t"
    "ds_read_b128 v[48:51], v16 offset:36864\n\t"
    "v_rcp_f32 v29, v29\n\t"
    "v_mul_f32 v28, v232, v28\n\t"
    "v_mul_f32 v33, s76, v237\n\t"
    "v_exp_f32 v32, v32\n\t"
    "v_fma_f32 v31, v31, s77, s77\n\t"
    "v_mfma_f32_32x32x16_bf16 v[176:191], a[244:247], a[28:31], v[176:191]\n\t"
    "v_rcp_f32 v30, v30\n\t"
    "v_mul_f32 v29, v233, v29\n\t"
    "v_cvt_pk_bf16_f32 v232, v28, v29\n\t"
    "v_mul_f32 v34, s76, v238\n\t"
    "v_exp_f32 v33, v33\n\t"
    "v_fma_f32 v32, v32, s77, s77\n\t"
    "v_rcp_f32 v31, v31\n\t"
    "v_mfma_f32_32x32x16_bf16 v[240:255], a[244:247], a[188:191], v[240:255]\n\t"
    "ds_read_b128 v[52:55], v16 offset:38912\n\t"
    "v_mul_f32 v30, v234, v30\n\t"
    "v_mul_f32 v35, s76, v239\n\t"
    "v_exp_f32 v34, v34\n\t"
    "v_fma_f32 v33, v33, s77, s77\n\t"
    "v_rcp_f32 v32, v32\n\t"
    "v_mul_f32 v31, v235, v31\n\t"
    "s_waitcnt lgkmcnt(3)\n\t"
    "v_mfma_f32_32x32x16_bf16 v[176:191], a[248:251], a[64:67], v[176:191]\n\t"
    "global_store_dwordx4 v15, v[168:171], s[50:51] offset:0 sc0 sc1 nt\n\t"
    "v_cvt_pk_bf16_f32 v233, v30, v31\n\t"
    "v_exp_f32 v35, v35\n\t"
    "v_mfma_f32_32x32x16_bf16 v[240:255], a[248:251], a[224:227], v[240:255]\n\t"
    "v_fma_f32 v34, v34, s77, s77\n\t"
    "v_rcp_f32 v33, v33\n\t"
    "v_mul_f32 v32, v236, v32\n\t"
    "v_fma_f32 v35, v35, s77, s77\n\t"
    "v_rcp_f32 v34, v34\n\t"
    "v_mul_f32 v33, v237, v33\n\t"
    "v_cvt_pk_bf16_f32 v234, v32, v33\n\t"
    "v_mfma_f32_32x32x16_bf16 v[176:191], a[252:255], a[68:71], v[176:191]\n\t"
    "v_rcp_f32 v35, v35\n\t"
    "v_mul_f32 v34, v238, v34\n\t"
    "v_mul_f32 v35, v239, v35\n\t"
    "v_cvt_pk_bf16_f32 v235, v34, v35\n\t"
    "v_mfma_f32_32x32x16_bf16 v[240:255], a[252:255], a[228:231], v[240:255]\n\t"
    "ds_write_b128 v19, v[232:235] offset:18432\n\t"
    "s_waitcnt lgkmcnt(1)\n\t"
    "v_mfma_f32_32x32x16_bf16 v[176:191], v[48:51], a[72:75], v[176:191]\n\t"
    "s_waitcnt lgkmcnt(0)\n\t"
    "s_barrier\n\t"
    "v_mfma_f32_32x32x16_bf16 v[240:255], v[48:51], a[232:235], v[240:255]\n\t"
    "ds_read_b128 a[240:243], v16 offset:16384\n\t"
    "ds_read_b128 a[244:247], v16 offset:18432\n\t"
    "ds_read_b128 a[248:251], v16 offset:20480\n\t"
    "v_mfma_f32_32x32x16_bf16 v[176:191], v[52:55], a[76:79], v[176:191]\n\t"
    "v_mfma_f32_32x32x16_bf16 v[240:255], v[52:55], a[236:239], v[240:255]\n\t"
    "ds_read_b128 a[252:255], v16 offset:22528\n\t"
    "s_waitcnt lgkmcnt(3)\n\t"
    "v_mfma_f32_32x32x16_bf16 v[176:191], a[240:243], a[32:35], v[176:191]\n\t"
    "v_mfma_f32_32x32x16_bf16 v[240:255], a[240:243], a[192:195], v[240:255]\n\t"
    "ds_read_b128 v[48:51], v16 offset:24576\n\t"
    "s_waitcnt lgkmcnt(2)\n\t"
    "v_mfma_f32_32x32x16_bf16 v[176:191], a[244:247], a[36:39], v[176:191]\n\t"
    "v_mfma_f32_32x32x16_bf16 v[240:255], a[244:247], a[196:199], v[240:255]\n\t"
    "ds_read_b128 v[52:55], v16 offset:26624\n\t"
    "v_mfma_f32_32x32x16_bf16 v[176:191], a[248:251], a[40:43], v[176:191]\n\t"
    "v_mfma_f32_32x32x16_bf16 v[240:255], a[248:251], a[200:203], v[240:255]\n\t"
    "ds_read_b128 a[240:243], v16 offset:28672\n\t"
    "s_waitcnt lgkmcnt(2)\n\t"
    "v_mfma_f32_32x32x16_bf16 v[176:191], a[252:255], a[44:47], v[176:191]\n\t"
    "v_mfma_f32_32x32x16_bf16 v[240:255], a[252:255], a[204:207], v[240:255]\n\t"
    "ds_read_b128 a[244:247], v16 offset:30720\n\t"
    "v_mfma_f32_32x32x16_bf16 v[176:191], v[48:51], a[48:51], v[176:191]\n\t"
    "v_mfma_f32_32x32x16_bf16 v[240:255], v[48:51], a[208:211], v[240:255]\n\t"
    "ds_read_b128 a[248:251], v17 offset:0\n\t"
    "s_waitcnt lgkmcnt(2)\n\t"
    "v_mfma_f32_32x32x16_bf16 v[176:191], v[52:55], a[52:55], v[176:191]\n\t"
    "ds_read_b128 a[252:255], v17 offset:2048\n\t"
    "v_mfma_f32_32x32x16_bf16 v[240:255], v[52:55], a[212:215], v[240:255]\n\t"
    "ds_read_b128 v[48:51], v17 offset:4096\n\t"
    "v_mfma_f32_32x32x16_bf16 v[176:191], a[240:243], a[56:59], v[176:191]\n\t"
    "ds_read_b128 v[52:55], v17 offset:6144\n\t"
    "v_mfma_f32_32x32x16_bf16 v[240:255], a[240:243], a[216:219], v[240:255]\n\t"
    "ds_read_b128 a[240:243], v17 offset:8192\n\t"
    "s_waitcnt lgkmcnt(4)\n\t"
    "v_mfma_f32_32x32x16_bf16 v[176:191], a[244:247], a[60:63], v[176:191]\n\t"
    "v_mfma_f32_32x32x16_bf16 v[240:255], a[244:247], a[220:223], v[240:255]\n\t"
    "ds_read_b128 a[244:247], v17 offset:10240\n\t"
    "v_mfma_f32_32x32x16_bf16 v[192:207], a[248:251], a[0:3], v[192:207]\n\t"
    "ds_read_b128 a[248:251], v17 offset:12288\n\t"
    "s_waitcnt lgkmcnt(4)\n\t"
    "v_mfma_f32_32x32x16_bf16 v[192:207], a[252:255], a[4:7], v[192:207]\n\t"
    "ds_read_b128 a[252:255], v17 offset:14336\n\t"
    "s_nop 4\n\t"
    "v_cvt_pk_bf16_f32 v176, v176, v177\n\t"
    "v_cvt_pk_bf16_f32 v177, v178, v179\n\t"
    "v_cvt_pk_bf16_f32 v178, v180, v181\n\t"
    "v_mfma_f32_32x32x16_bf16 v[192:207], v[48:51], a[8:11], v[192:207]\n\t"
    "ds_read_b128 v[48:51], v17 offset:16384\n\t"
    "v_cvt_pk_bf16_f32 v179, v182, v183\n\t"
    "v_cvt_pk_bf16_f32 v184, v184, v185\n\t"
    "v_cvt_pk_bf16_f32 v185, v186, v187\n\t"
    "s_waitcnt lgkmcnt(4)\n\t"
    "v_mfma_f32_32x32x16_bf16 v[192:207], v[52:55], a[12:15], v[192:207]\n\t"
    "ds_read_b128 v[52:55], v17 offset:18432\n\t"
    "v_cvt_pk_bf16_f32 v186, v188, v189\n\t"
    "v_cvt_pk_bf16_f32 v187, v190, v191\n\t"
    "v_permlane16_swap_b32 v176, v184\n\t"
    "v_mfma_f32_32x32x16_bf16 v[192:207], a[240:243], a[16:19], v[192:207]\n\t"
    "ds_read_b128 a[240:243], v17 offset:20480\n\t"
    "v_permlane16_swap_b32 v177, v185\n\t"
    "v_permlane16_swap_b32 v178, v186\n\t"
    "v_permlane16_swap_b32 v179, v187\n\t"
    "s_waitcnt lgkmcnt(4)\n\t"
    "v_mfma_f32_32x32x16_bf16 v[192:207], a[244:247], a[20:23], v[192:207]\n\t"
    "ds_read_b128 a[244:247], v17 offset:22528\n\t"
    "global_store_dwordx4 v15, v[176:179], s[40:41] offset:128 sc0 sc1 nt\n\t"
    "v_mfma_f32_32x32x16_bf16 v[192:207], a[248:251], a[24:27], v[192:207]\n\t"
    ".Ledm_hop3_%=:\n\t"
    "s_cbranch_execz .Ledm_ret%=\n\t"
    "ds_read_b128 a[248:251], v17 offset:24576\n\t"
    "s_waitcnt lgkmcnt(4)\n\t"
    "v_mfma_f32_32x32x16_bf16 v[192:207], a[252:255], a[28:31], v[192:207]\n\t"
    "ds_read_b128 a[252:255], v17 offset:26624\n\t"
    "v_mfma_f32_32x32x16_bf16 v[192:207], v[48:51], a[32:35], v[192:207]\n\t"
    "ds_read_b128 v[48:51], v17 offset:28672\n\t"
    "s_waitcnt lgkmcnt(4)\n\t"
    "v_mfma_f32_32x32x16_bf16 v[192:207], v[52:55], a[36:39], v[192:207]\n\t"
    "ds_read_b128 v[52:55], v17 offset:30720\n\t"
    "global_store_dwordx4 v15, v[184:187], s[42:43] offset:128 sc0 sc1 nt\n\t"
    "v_mfma_f32_32x32x16_bf16 v[192:207], a[240:243], a[40:43], v[192:207]\n\t"
    "ds_read_b128 a[240:243], v17 offset:32768\n\t"
    "v_cvt_pk_bf16_f32 v240, v240, v241\n\t"
    "v_cvt_pk_bf16_f32 v241, v242, v243\n\t"
    "v_cvt_pk_bf16_f32 v242, v244, v245\n\t"
    "v_cvt_pk_bf16_f32 v243, v246, v247\n\t"
    "s_waitcnt lgkmcnt(4)\n\t"
    "v_mfma_f32_32x32x16_bf16 v[192:207], a[244:247], a[44:47], v[192:207]\n\t"
    "ds_read_b128 a[244:247], v17 offset:34816\n\t"
    "v_cvt_pk_bf16_f32 v248, v248, v249\n\t"
    "v_cvt_pk_bf16_f32 v249, v250, v251\n\t"
    "v_cvt_pk_bf16_f32 v250, v252, v253\n\t"
    "v_cvt_pk_bf16_f32 v251, v254, v255\n\t"
    "v_mfma_f32_32x32x16_bf16 v[192:207], a[248:251], a[48:51], v[192:207]\n\t"
    "ds_read_b128 a[248:251], v17 offset:36864\n\t"
    "v_permlane16_swap_b32 v240, v248\n\t"
    "v_permlane16_swap_b32 v241, v249\n\t"
    "v_permlane16_swap_b32 v242, v250\n\t"
    "v_permlane16_swap_b32 v243, v251\n\t"
    "s_waitcnt lgkmcnt(4)\n\t"
    "v_mfma_f32_32x32x16_bf16 v[192:207], a[252:255], a[52:55], v[192:207]\n\t"
    "ds_read_b128 a[252:255], v17 offset:38912\n\t"
    "global_store_dwordx4 v15, v[240:243], s[48:49] offset:128 sc0 sc1 nt\n\t"
    "v_mfma_f32_32x32x16_bf16 v[192:207], v[48:51], a[56:59], v[192:207]\n\t"
    "s_waitcnt lgkmcnt(3)\n\t"
    "v_mfma_f32_32x32x16_bf16 v[192:207], v[52:55], a[60:63], v[192:207]\n\t"
    "ds_read_b128 v[48:51], v16 offset:0\n\t"
    "v_mfma_f32_32x32x16_bf16 v[192:207], a[240:243], a[64:67], v[192:207]\n\t"
    "ds_read_b128 v[52:55], v16 offset:2048\n\t"
    "s_waitcnt lgkmcnt(3)\n\t"
    "v_mfma_f32_32x32x16_bf16 v[192:207], a[244:247], a[68:71], v[192:207]\n\t"
    "ds_read_b128 a[240:243], v16 offset:4096\n\t"
    "global_store_dwordx4 v15, v[248:251], s[50:51] offset:128 sc0 sc1 nt\n\t"
    "v_mfma_f32_32x32x16_bf16 v[192:207], a[248:251], a[72:75], v[192:207]\n\t"
    "ds_read_b128 a[244:247], v16 offset:6144\n\t"
    "s_waitcnt lgkmcnt(3)\n\t"
    "v_mfma_f32_32x32x16_bf16 v[192:207], a[252:255], a[76:79], v[192:207]\n\t"
    "ds_read_b128 a[248:251], v16 offset:8192\n\t"
    "v_mfma_f32_32x32x16_bf16 v[208:223], v[72:75], v[36:39], 0\n\t"
    "ds_read_b128 a[252:255], v16 offset:10240\n\t"
    "v_mfma_f32_32x32x16_bf16 v[208:223], v[76:79], v[40:43], v[208:223]\n\t"
    "v_mfma_f32_32x32x16_bf16 v[208:223], v[48:51], a[80:83], v[208:223]\n\t"
    "ds_read_b128 v[48:51], v16 offset:12288\n\t"
    "s_nop 5\n\t"
    "v_cvt_pk_bf16_f32 v192, v192, v193\n\t"
    "v_cvt_pk_bf16_f32 v193, v194, v195\n\t"
    "v_cvt_pk_bf16_f32 v194, v196, v197\n\t"
    "s_waitcnt lgkmcnt(4)\n\t"
    "v_mfma_f32_32x32x16_bf16 v[208:223], v[52:55], a[84:87], v[208:223]\n\t"
    "ds_read_b128 v[52:55], v16 offset:14336\n\t"
    "v_cvt_pk_bf16_f32 v195, v198, v199\n\t"
    "v_cvt_pk_bf16_f32 v200, v200, v201\n\t"
    "v_cvt_pk_bf16_f32 v201, v202, v203\n\t"
    "v_mfma_f32_32x32x16_bf16 v[208:223], a[240:243], a[88:91], v[208:223]\n\t"
    "ds_read_b128 a[240:243], v16 offset:16384\n\t"
    "v_cvt_pk_bf16_f32 v202, v204, v205\n\t"
    "v_cvt_pk_bf16_f32 v203, v206, v207\n\t"
    "v_permlane16_swap_b32 v192, v200\n\t"
    "s_waitcnt lgkmcnt(4)\n\t"
    "v_mfma_f32_32x32x16_bf16 v[208:223], a[244:247], a[92:95], v[208:223]\n\t"
    "ds_read_b128 a[244:247], v16 offset:18432\n\t"
    "v_permlane16_swap_b32 v193, v201\n\t"
    "v_permlane16_swap_b32 v194, v202\n\t"
    "v_permlane16_swap_b32 v195, v203\n\t"
    "v_mfma_f32_32x32x16_bf16 v[208:223], a[248:251], a[96:99], v[208:223]\n\t"
    "ds_read_b128 a[248:251], v16 offset:20480\n\t"
    "global_store_dwordx4 v25, v[192:195], s[40:41] offset:128 sc0 sc1 nt\n\t"
    "s_waitcnt lgkmcnt(4)\n\t"
    "v_mfma_f32_32x32x16_bf16 v[208:223], a[252:255], a[100:103], v[208:223]\n\t"
    "ds_read_b128 a[252:255], v16 offset:22528\n\t"
    "v_mfma_f32_32x32x16_bf16 v[208:223], v[48:51], a[104:107], v[208:223]\n\t"
    "ds_read_b128 v[48:51], v16 offset:24576\n\t"
    "s_waitcnt lgkmcnt(4)\n\t"
    "v_mfma_f32_32x32x16_bf16 v[208:223], v[52:55], a[108:111], v[208:223]\n\t"
    "ds_read_b128 v[52:55], v16 offset:26624\n\t"
    "v_mfma_f32_32x32x16_bf16 v[208:223], a[240:243], a[112:115], v[208:223]\n\t"
    "ds_read_b128 a[240:243], v16 offset:28672\n\t"
    "global_store_dwordx4 v25, v[200:203], s[42:43] offset:128 sc0 sc1 nt\n\t"
    "s_waitcnt lgkmcnt(4)\n\t"
    "v_mfma_f32_32x32x16_bf16 v[208:223], a[244:247], a[116:119], v[208:223]\n\t"
    "ds_read_b128 a[244:247], v16 offset:30720\n\t"
    "v_mfma_f32_32x32x16_bf16 v[208:223], a[248:251], a[120:123], v[208:223]\n\t"
    "ds_read_b128 a[248:251], v16 offset:32768\n\t"
    "s_waitcnt lgkmcnt(4)\n\t"
    "v_mfma_f32_32x32x16_bf16 v[208:223], a[252:255], a[124:127], v[208:223]\n\t"
    "ds_read_b128 a[252:255], v16 offset:34816\n\t"
    "v_mfma_f32_32x32x16_bf16 v[208:223], v[48:51], a[128:131], v[208:223]\n\t"
    "ds_read_b128 v[48:51], v16 offset:36864\n\t"
    "s_waitcnt lgkmcnt(4)\n\t"
    "v_mfma_f32_32x32x16_bf16 v[208:223], v[52:55], a[132:135], v[208:223]\n\t"
    "ds_read_b128 v[52:55], v16 offset:38912\n\t"
    "v_mfma_f32_32x32x16_bf16 v[208:223], a[240:243], a[136:139], v[208:223]\n\t"
    "s_waitcnt lgkmcnt(3)\n\t"
    "v_mfma_f32_32x32x16_bf16 v[208:223], a[244:247], a[140:143], v[208:223]\n\t"
    "ds_read_b128 a[240:243], v17 offset:0\n\t"
    "v_mfma_f32_32x32x16_bf16 v[208:223], a[248:251], a[144:147], v[208:223]\n\t"
    "ds_read_b128 a[244:247], v17 offset:2048\n\t"
    "s_waitcnt lgkmcnt(3)\n\t"
    "v_mfma_f32_32x32x16_bf16 v[208:223], a[252:255], a[148:151], v[208:223]\n\t"
    "ds_read_b128 a[248:251], v17 offset:4096\n\t"
    "v_mfma_f32_32x32x16_bf16 v[208:223], v[48:51], a[152:155], v[208:223]\n\t"
    "ds_read_b128 a[252:255], v17 offset:6144\n\t"
    "s_waitcnt lgkmcnt(3)\n\t"
    "v_mfma_f32_32x32x16_bf16 v[208:223], v[52:55], a[156:159], v[208:223]\n\t"
    "ds_read_b128 v[48:51], v17 offset:8192\n\t"
    "v_mfma_f32_32x32x16_bf16 v[224:239], v[80:83], v[36:39], 0\n\t"
    "ds_read_b128 v[52:55], v17 offset:10240\n\t"
    "v_mfma_f32_32x32x16_bf16 v[224:239], v[84:87], v[40:43], v[224:239]\n\t"
    "v_mfma_f32_32x32x16_bf16 v[224:239], a[240:243], a[80:83], v[224:239]\n\t"
    "ds_read_b128 a[240:243], v17 offset:12288\n\t"
    "s_nop 5\n\t"
    "v_cvt_pk_bf16_f32 v208, v208, v209\n\t"
    "v_cvt_pk_bf16_f32 v209, v210, v211\n\t"
    "v_cvt_pk_bf16_f32 v210, v212, v213\n\t"
    "s_waitcnt lgkmcnt(4)\n\t"
    "v_mfma_f32_32x32x16_bf16 v[224:239], a[244:247], a[84:87], v[224:239]\n\t"
    "ds_read_b128 a[244:247], v17 offset:14336\n\t"
    "v_cvt_pk_bf16_f32 v211, v214, v215\n\t"
    "v_cvt_pk_bf16_f32 v216, v216, v217\n\t"
    "v_cvt_pk_bf16_f32 v217, v218, v219\n\t"
    "v_mfma_f32_32x32x16_bf16 v[224:239], a[248:251], a[88:91], v[224:239]\n\t"
    "ds_read_b128 a[248:251], v17 offset:16384\n\t"
    "v_cvt_pk_bf16_f32 v218, v220, v221\n\t"
    "v_cvt_pk_bf16_f32 v219, v222, v223\n\t"
    "v_permlane16_swap_b32 v208, v216\n\t"
    "s_waitcnt lgkmcnt(4)\n\t"
    "v_mfma_f32_32x32x16_bf16 v[224:239], a[252:255], a[92:95], v[224:239]\n\t"
    "ds_read_b128 a[252:255], v17 offset:18432\n\t"
    "v_permlane16_swap_b32 v209, v217\n\t"
    "v_permlane16_swap_b32 v210, v218\n\t"
    "v_permlane16_swap_b32 v211, v219\n\t"
    "v_mfma_f32_32x32x16_bf16 v[224:239], v[48:51], a[96:99], v[224:239]\n\t"
    "ds_read_b128 v[48:51], v17 offset:20480\n\t"
    "global_store_dwordx4 v15, v[208:211], s[44:45] offset:128 sc0 sc1 nt\n\t"
    "s_waitcnt lgkmcnt(4)\n\t"
    "v_mfma_f32_32x32x16_bf16 v[224:239], v[52:55], a[100:103], v[224:239]\n\t"
    "ds_read_b128 v[52:55], v17 offset:22528\n\t"
    "v_mfma_f32_32x32x16_bf16 v[224:239], a[240:243], a[104:107], v[224:239]\n\t"
    "ds_read_b128 a[240:243], v17 offset:24576\n\t"
    "s_waitcnt lgkmcnt(4)\n\t"
    "v_mfma_f32_32x32x16_bf16 v[224:239], a[244:247], a[108:111], v[224:239]\n\t"
    "ds_read_b128 a[244:247], v17 offset:26624\n\t"
    "v_mfma_f32_32x32x16_bf16 v[224:239], a[248:251], a[112:115], v[224:239]\n\t"
    "ds_read_b128 a[248:251], v17 offset:28672\n\t"
    "global_store_dwordx4 v15, v[216:219], s[46:47] offset:128 sc0 sc1 nt\n\t"
    "s_waitcnt lgkmcnt(4)\n\t"
    "v_mfma_f32_32x32x16_bf16 v[224:239], a[252:255], a[116:119], v[224:239]\n\t"
    "ds_read_b128 a[252:255], v17 offset:30720\n\t"
    "v_mfma_f32_32x32x16_bf16 v[224:239], v[48:51], a[120:123], v[224:239]\n\t"
    "ds_read_b128 v[48:51], v17 offset:32768\n\t"
    "s_waitcnt lgkmcnt(4)\n\t"
    "v_mfma_f32_32x32x16_bf16 v[224:239], v[52:55], a[124:127], v[224:239]\n\t"
    "ds_read_b128 v[52:55], v17 offset:34816\n\t"
    "v_mfma_f32_32x32x16_bf16 v[224:239], a[240:243], a[128:131], v[224:239]\n\t"
    "ds_read_b128 a[240:243], v17 offset:36864\n\t"
    "s_waitcnt lgkmcnt(4)\n\t"
    "v_mfma_f32_32x32x16_bf16 v[224:239], a[244:247], a[132:135], v[224:239]\n\t"
    "ds_read_b128 a[244:247], v17 offset:38912\n\t"
    "v_mfma_f32_32x32x16_bf16 v[224:239], a[248:251], a[136:139], v[224:239]\n\t"
    "s_waitcnt lgkmcnt(3)\n\t"
    "v_mfma_f32_32x32x16_bf16 v[224:239], a[252:255], a[140:143], v[224:239]\n\t"
    "v_mfma_f32_32x32x16_bf16 v[224:239], v[48:51], a[144:147], v[224:239]\n\t"
    "s_waitcnt lgkmcnt(1)\n\t"
    "v_mfma_f32_32x32x16_bf16 v[224:239], v[52:55], a[148:151], v[224:239]\n\t"
    "v_mfma_f32_32x32x16_bf16 v[224:239], a[240:243], a[152:155], v[224:239]\n\t"
    "s_waitcnt lgkmcnt(0)\n\t"
    "v_mfma_f32_32x32x16_bf16 v[224:239], a[244:247], a[156:159], v[224:239]\n\t"
    "s_nop 11\n\t"
    "v_cvt_pk_bf16_f32 v224, v224, v225\n\t"
    "v_cvt_pk_bf16_f32 v225, v226, v227\n\t"
    "v_cvt_pk_bf16_f32 v226, v228, v229\n\t"
    "v_cvt_pk_bf16_f32 v227, v230, v231\n\t"
    "v_cvt_pk_bf16_f32 v232, v232, v233\n\t"
    "v_cvt_pk_bf16_f32 v233, v234, v235\n\t"
    "v_cvt_pk_bf16_f32 v234, v236, v237\n\t"
    "v_cvt_pk_bf16_f32 v235, v238, v239\n\t"
    "v_permlane16_swap_b32 v224, v232\n\t"
    "v_permlane16_swap_b32 v225, v233\n\t"
    "v_permlane16_swap_b32 v226, v234\n\t"
    "v_permlane16_swap_b32 v227, v235\n\t"
    "global_store_dwordx4 v25, v[224:227], s[44:45] offset:128 sc0 sc1 nt\n\t"
    "global_store_dwordx4 v25, v[232:235], s[46:47] offset:128 sc0 sc1 nt\n\t"
    "s_endpgm\n\t"
    ".rept 272\n\t"
    "s_nop 0\n\t"
    ".endr\n\t"
    ".Ledm_codeend%=:\n\t"
    :: [ka]"s"(__builtin_amdgcn_kernarg_segment_ptr()), [blk]"s"(blockIdx.x), [tid]"v"(threadIdx.x)
    : "v1","v2","v3","v4","v5","v6","v7","v8","v9","v10","v11","v12","v13","v14","v15","v16","v17","v18","v19","v20","v21","v22","v23","v24","v25","v26","v27","v28","v29","v30","v31","v32","v33","v34","v35","v36","v37","v38","v39","v40","v41","v42","v43","v44","v45","v46","v47","v48","v49","v50","v51","v52","v53","v54","v55","v56","v57","v58","v59","v60","v61","v62","v63","v64","v65","v66","v67","v68","v69","v70","v71","v72","v73","v74","v75","v76","v77","v78","v79","v80","v81","v82","v83","v84","v85","v86","v87","v88","v89","v90","v91","v92","v93","v94","v95","v96","v97","v98","v99","v100","v101","v102","v103","v104","v105","v106","v107","v108","v109","v110","v111","v112","v113","v114","v115","v116","v117","v118","v119","v120","v121","v122","v123","v124","v125","v126","v127","v128","v129","v130","v131","v132","v133","v134","v135","v136","v137","v138","v139","v140","v141","v142","v143","v144","v145","v146","v147","v148","v149","v150","v151","v152","v153","v154","v155","v156","v157","v158","v159","v160","v161","v162","v163","v164","v165","v166","v167","v168","v169","v170","v171","v172","v173","v174","v175","v176","v177","v178","v179","v180","v181","v182","v183","v184","v185","v186","v187","v188","v189","v190","v191","v192","v193","v194","v195","v196","v197","v198","v199","v200","v201","v202","v203","v204","v205","v206","v207","v208","v209","v210","v211","v212","v213","v214","v215","v216","v217","v218","v219","v220","v221","v222","v223","v224","v225","v226","v227","v228","v229","v230","v231","v232","v233","v234","v235","v236","v237","v238","v239","v240","v241","v242","v243","v244","v245","v246","v247","v248","v249","v250","v251","v252","v253","v254","v255","a0","a1","a2","a3","a4","a5","a6","a7","a8","a9","a10","a11","a12","a13","a14","a15","a16","a17","a18","a19","a20","a21","a22","a23","a24","a25","a26","a27","a28","a29","a30","a31","a32","a33","a34","a35","a36","a37","a38","a39","a40","a41","a42","a43","a44","a45","a46","a47","a48","a49","a50","a51","a52","a53","a54","a55","a56","a57","a58","a59","a60","a61","a62","a63","a64","a65","a66","a67","a68","a69","a70","a71","a72","a73","a74","a75","a76","a77","a78","a79","a80","a81","a82","a83","a84","a85","a86","a87","a88","a89","a90","a91","a92","a93","a94","a95","a96","a97","a98","a99","a100","a101","a102","a103","a104","a105","a106","a107","a108","a109","a110","a111","a112","a113","a114","a115","a116","a117","a118","a119","a120","a121","a122","a123","a124","a125","a126","a127","a128","a129","a130","a131","a132","a133","a134","a135","a136","a137","a138","a139","a140","a141","a142","a143","a144","a145","a146","a147","a148","a149","a150","a151","a152","a153","a154","a155","a156","a157","a158","a159","a160","a161","a162","a163","a164","a165","a166","a167","a168","a169","a170","a171","a172","a173","a174","a175","a176","a177","a178","a179","a180","a181","a182","a183","a184","a185","a186","a187","a188","a189","a190","a191","a192","a193","a194","a195","a196","a197","a198","a199","a200","a201","a202","a203","a204","a205","a206","a207","a208","a209","a210","a211","a212","a213","a214","a215","a216","a217","a218","a219","a220","a221","a222","a223","a224","a225","a226","a227","a228","a229","a230","a231","a232","a233","a234","a235","a236","a237","a238","a239","a240","a241","a242","a243","a244","a245","a246","a247","a248","a249","a250","a251","a252","a253","a254","a255","s20","s21","s22","s23","s24","s25","s26","s27","s28","s29","s30","s31","s33","s34","s35","s36","s37","s38","s39","s40","s41","s42","s43","s44","s45","s46","s47","s48","s49","s50","s51","s52","s53","s54","s55","s56","s57","s58","s59","s60","s61","s62","s63","s64","s65","s66","s67","s68","s69","s70","s71","s72","s73","s74","s75","s76","s77","s78","s79","s80","s81","s82","s83","s84","s85","s86","s87","s88","s89","s90","s91","s92","s93","s94","s95","s96","s97","s98","s99","vcc","scc","memory");
}

extern "C" void kernel_launch(void* const* d_in, const int* in_sizes, int n_in,
                              void* d_out, int out_size, void* d_ws, size_t ws_size,
                              hipStream_t stream) {
  (void)hipFuncSetAttribute((const void*)edm_kernel, hipFuncAttributeMaxDynamicSharedMemorySize, 163840);
  edm_kernel<<<256, 256, 163840, stream>>>(d_in[0], d_in[1], d_in[2], d_in[3], d_in[4], d_out);
}
```
